```python
import torch

def _softmax(x, axis=-1):
    return torch.softmax(x, dim=axis)
def _gelu(x):
    return torch.nn.functional.gelu(x, approximate="tanh")
def _silu(x):
    return torch.nn.functional.silu(x)
def _sigmoid(x):
    return torch.sigmoid(x)
def _layernorm(x, axis=-1, eps=1e-5):
    m = x.mean(dim=axis, keepdim=True)
    v = x.var(dim=axis, keepdim=True, unbiased=False)
    return (x - m) / torch.sqrt(v + eps)
def _scatter_add(tgt, idx, src, axis=0):
    return tgt.clone().index_add_(axis, idx, src)
def _segment_sum(src, seg, n):
    out = torch.zeros((n,) + src.shape[1:], dtype=src.dtype)
    return out.index_add_(0, seg, src)
def _pad(x, pw):
    flat = tuple(v for pair in reversed(pw) for v in pair)
    return torch.nn.functional.pad(x, flat)

kernel_name = 'sequential_preLN__softmax_full_mha__geglu_ffn__prompt'

def setup_inputs():

    _w = (lambda a: (lambda t: t.to(torch.bfloat16) if t.is_floating_point() else t)(torch.as_tensor(a)))
    def _A_setup():
        B, S, D = 1, 2048, 448
        return {
            "x": _w((lambda *s: torch.randn(*s, dtype=torch.bfloat16) * 0.05)(B, S, D)),
            "wqkv": _w((lambda *s: torch.randn(*s, dtype=torch.bfloat16) * 0.05)(D, 3 * D)),
            "wo": _w((lambda *s: torch.randn(*s, dtype=torch.bfloat16) * 0.05)(D, D)),
        }
    _A_in = _A_setup()

    def _B_setup():
        B, S, D, F = 1, 2048, 448, 1792
        return {
            "x": _w((lambda *s: torch.randn(*s, dtype=torch.bfloat16) * 0.05)(B, S, D)),
            "w_gate": _w((lambda *s: torch.randn(*s, dtype=torch.bfloat16) * 0.05)(D, F)),
            "w_up": _w((lambda *s: torch.randn(*s, dtype=torch.bfloat16) * 0.05)(D, F)),
            "w_down": _w((lambda *s: torch.randn(*s, dtype=torch.bfloat16) * 0.05)(F, D)),
        }
    _B_in = _B_setup()

    return {
        "A_x": _A_in["x"],
        "A_wqkv": _A_in["wqkv"],
        "A_wo": _A_in["wo"],
        "B_x": _B_in["x"],
        "B_w_gate": _B_in["w_gate"],
        "B_w_up": _B_in["w_up"],
        "B_w_down": _B_in["w_down"],
    }

def reference(A_x, A_wqkv, A_wo, B_x, B_w_gate, B_w_up, B_w_down):
    def _A_ref(x, wqkv, wo):
        B, S, D = x.shape
        H = 16; Dh = D // H
        qkv = (lambda x, *d: x.permute(*d))((x @ wqkv).reshape(B, S, 3, H, Dh), 2, 0, 3, 1, 4)
        q, k, v = qkv[0], qkv[1], qkv[2]
        scale = Dh ** -0.5
        scores = torch.einsum("bhqd,bhkd->bhqk", q, k) * scale
        p = _softmax(scores, axis=-1)
        o = torch.einsum("bhqk,bhkd->bhqd", p, v)
        return (lambda x, *d: x.permute(*d))(o, 0, 2, 1, 3).reshape(B, S, D) @ wo

    def _B_ref(x, w_gate, w_up, w_down):
        return (_gelu(x @ w_gate) * (x @ w_up)) @ w_down

    x = A_x
    h = x + _A_ref(_layernorm(x, axis=-1), A_wqkv, A_wo)
    h2 = h + _B_ref(_layernorm(h, axis=-1), B_w_gate, B_w_up, B_w_down)
    return h2
```

```cpp
#include <hip/hip_runtime.h>
#include <hip/hip_bf16.h>

typedef unsigned short u16;
typedef __attribute__((ext_vector_type(8))) short bf16x8;
typedef __attribute__((ext_vector_type(4))) short s16x4;
typedef __attribute__((ext_vector_type(2))) __bf16 b16x2;
typedef __attribute__((ext_vector_type(2))) float f32x2;
typedef __attribute__((ext_vector_type(4))) float f32x4;
typedef __attribute__((ext_vector_type(16))) float f32x16;
typedef __attribute__((ext_vector_type(4))) unsigned u32x4;
typedef __attribute__((ext_vector_type(2))) unsigned u32x2;
#define LAS __attribute__((address_space(3)))
#define DI __device__ __forceinline__
#define MFMA(a, b, c) __builtin_amdgcn_mfma_f32_32x32x16_bf16((a), (b), (c), 0, 0, 0)
#define WAIT_V(n) asm volatile("s_waitcnt vmcnt(" #n ") lgkmcnt(0)" ::: "memory")
#define BAR() __builtin_amdgcn_s_barrier()

constexpr int S_ = 2048, D_ = 448, H_ = 16, DH_ = 28, F_ = 1792, N3_ = 1344;
constexpr float LN_EPS = 1e-5f;
constexpr float QSCALE = 0.18898223650461362f * 1.4426950408889634f;
constexpr float SAFE_T2 = 3600.0f;

DI unsigned pk2(float lo, float hi) {
  f32x2 f = {lo, hi};
  return __builtin_bit_cast(unsigned, __builtin_convertvector(f, b16x2));
}
DI float bf_lo(unsigned u) { return __uint_as_float(u << 16); }
DI float bf_hi(unsigned u) { return __uint_as_float(u & 0xffff0000u); }
DI float dot2(unsigned a, unsigned b, float c) {
  return __builtin_amdgcn_fdot2_f32_bf16(__builtin_bit_cast(b16x2, a), __builtin_bit_cast(b16x2, b), c, false);
}
constexpr unsigned ONES2 = 0x3f803f80u;
DI void dma16(const void* sbase, int voff, unsigned lds) {
  unsigned keep;
  asm volatile("s_mov_b32 %0, m0\n\ts_mov_b32 m0, %3\n\ts_nop 0\n\tglobal_load_lds_dwordx4 %1, %2\n\ts_mov_b32 m0, %0"
               : "=&s"(keep) : "v"(voff), "s"(sbase), "s"(lds) : "memory");
}
DI unsigned lds_base(char* smem) { return (unsigned)(size_t)(LAS char*)smem; }
DI u32x4 lds_r128(const char* p) { return *(const u32x4*)p; }
DI u32x4 lds_tr2(const char* p0, const char* p1) {
  s16x4 a = __builtin_amdgcn_ds_read_tr16_b64_v4i16((LAS s16x4*)p0);
  s16x4 b = __builtin_amdgcn_ds_read_tr16_b64_v4i16((LAS s16x4*)p1);
  u32x2 ua = __builtin_bit_cast(u32x2, a), ub = __builtin_bit_cast(u32x2, b);
  u32x4 r = {ua[0], ua[1], ub[0], ub[1]};
  return r;
}
DI f32x16 mfma_u(u32x4 a, u32x4 b, f32x16 c) {
  return MFMA(__builtin_bit_cast(bf16x8, a), __builtin_bit_cast(bf16x8, b), c);
}
DI float xor32(float v) { return __shfl_xor(v, 32); }

constexpr int A_XB = 128 * 128, A_WB = 64 * 192, A_STG = A_XB + A_WB, A_NBUF = 5;
constexpr int A_MISC = A_STG * A_NBUF;
constexpr int A_LDS = A_MISC + 4 * 96 * 4 + 128 * 8;

template <int NV>
DI void qkv_body(const u16* __restrict__ x, const u16* __restrict__ w, u16* __restrict__ Qf, u16* __restrict__ Kf,
                 u16* __restrict__ Vf, float* __restrict__ qn, float* __restrict__ kn, char* smem, int mt, int nt) {
  const int tid = threadIdx.x, lane = tid & 63, wv = __builtin_amdgcn_readfirstlane(tid >> 6);
  const int r = lane & 31, h = lane >> 5;
  const int m0 = mt * 128, c0 = 84 * nt, ca = c0 & ~7, off = c0 - ca;
  int xsrc[2];
#pragma unroll
  for (int par = 0; par < 2; ++par) {
    int c = (lane & 7) ^ ((par * 4 + (lane >> 4)) & 7);
    xsrc[par] = ((m0 + 32 * wv + (lane >> 3)) * D_ + 8 * c) * 2;
  }
  int wsrc[3];
#pragma unroll
  for (int i = 0; i < 3; ++i) {
    int ci = 64 * (3 * wv + i) + lane, row = ci / 12, c = ci - row * 12;
    int col = ca + 8 * c;
    col = col > (N3_ - 8) ? (N3_ - 8) : col;
    wsrc[i] = (row * N3_ + col) * 2;
  }
  auto issue = [&](int s) {
    const unsigned buf = lds_base(smem) + (s % A_NBUF) * A_STG;
#pragma unroll
    for (int i = 0; i < 4; ++i)
      dma16(x + i * 8 * D_ + 64 * s, xsrc[i & 1], buf + (32 * wv + 8 * i) * 128);
#pragma unroll
    for (int i = 0; i < 3; ++i)
      dma16(w + 64 * s * N3_, wsrc[i], buf + A_XB + (3 * wv + i) * 1024);
  };
#pragma unroll
  for (int s = 0; s < 5; ++s) issue(s);

  f32x16 acc[3];
#pragma unroll
  for (int b = 0; b < 3; ++b)
#pragma unroll
    for (int i = 0; i < 16; ++i) acc[b][i] = 0.f;
  float sum = 0.f, sq = 0.f, cs[3] = {0.f, 0.f, 0.f};
  const int xrow = 32 * wv + r;
  const int xoff = xrow * 128, xsw = (xrow >> 1) & 7;
  const int q4 = (lane & 15) >> 2, p4 = lane & 3, blk = (lane >> 4) & 1;
  const int woff = A_XB + (8 * h + q4) * 192 + (off + 16 * blk + 4 * p4) * 2;

#pragma unroll
  for (int s = 0; s < 7; ++s) {
    if (s == 0) WAIT_V(28);
    else if (s <= 3) WAIT_V(21);
    else if (s == 4) WAIT_V(14);
    else if (s == 5) WAIT_V(7);
    else WAIT_V(0);
    BAR();
    if (s == 1 || s == 2) issue(s + 4);
    const char* buf = smem + (s % A_NBUF) * A_STG;
#pragma unroll
    for (int ks = 0; ks < 4; ++ks) {
      u32x4 xf = lds_r128(buf + xoff + (((2 * ks + h) ^ xsw) << 4));
      u32x4 wf[3];
#pragma unroll
      for (int b = 0; b < 3; ++b) {
        const char* p = buf + woff + ks * 16 * 192 + b * 56;
        wf[b] = lds_tr2(p, p + 4 * 192);
      }
#pragma unroll
      for (int b = 0; b < 3; ++b) {
        if (b >= 3 - NV) acc[b] = mfma_u(xf, wf[b], acc[b]);
        else acc[b] = mfma_u(wf[b], xf, acc[b]);
      }
#pragma unroll
      for (int i = 0; i < 4; ++i) { sum = dot2(xf[i], ONES2, sum); sq = dot2(xf[i], xf[i], sq); }
      if (ks == wv) {
#pragma unroll
        for (int b = 0; b < 3; ++b)
#pragma unroll
          for (int i = 0; i < 4; ++i) cs[b] = dot2(wf[b][i], ONES2, cs[b]);
      }
    }
  }
  float* csp = (float*)(smem + A_MISC);
  float* tst = (float*)(smem + A_MISC + 4 * 96 * 4);
  sum += xor32(sum); sq += xor32(sq);
  const float mean = sum * (1.0f / D_);
  float var = sq * (1.0f / D_) - mean * mean;
  var = var > 0.f ? var : 0.f;
  const float rstd = rsqrtf(var + LN_EPS);
#pragma unroll
  for (int b = 0; b < 3; ++b) cs[b] += xor32(cs[b]);
  if (h == 0) {
#pragma unroll
    for (int b = 0; b < 3; ++b) csp[wv * 96 + b * 32 + r] = cs[b];
    if (NV > 0) { tst[(32 * wv + r) * 2] = mean; tst[(32 * wv + r) * 2 + 1] = rstd; }
  }
  __syncthreads();
  const int tb = mt * 4 + wv;
#pragma unroll
  for (int b = 0; b < 3; ++b) {
    const int gb = 3 * nt + b, head = gb & 15, type = gb >> 4;
    unsigned o[8];
    if (b >= 3 - NV) {
      float c = csp[b * 32 + r] + csp[96 + b * 32 + r] + csp[192 + b * 32 + r] + csp[288 + b * 32 + r];
#pragma unroll
      for (int g = 0; g < 4; ++g) {
        const float* sp = tst + (32 * wv + 8 * g + 4 * h) * 2;
        f32x4 s0 = *(const f32x4*)sp, s1 = *(const f32x4*)(sp + 4);
        float v0 = s0[1] * (acc[b][4 * g] - s0[0] * c), v1 = s0[3] * (acc[b][4 * g + 1] - s0[2] * c);
        float v2 = s1[1] * (acc[b][4 * g + 2] - s1[0] * c), v3 = s1[3] * (acc[b][4 * g + 3] - s1[2] * c);
        o[2 * g] = pk2(v0, v1); o[2 * g + 1] = pk2(v2, v3);
      }
      if (r >= DH_) {
        const unsigned f = (r == DH_) ? ONES2 : 0u;
#pragma unroll
        for (int i = 0; i < 8; ++i) o[i] = f;
      }
      u16* dst = Vf + ((size_t)(head * 64 + tb) * 2 * 64 + lane) * 8;
      *(u32x4*)dst = (u32x4){o[0], o[1], o[2], o[3]};
      *(u32x4*)(dst + 512) = (u32x4){o[4], o[5], o[6], o[7]};
    } else {
      const float sc = type == 0 ? rstd * QSCALE : rstd;
      float n2 = 0.f;
#pragma unroll
      for (int g = 0; g < 4; ++g) {
        const float* cp = csp + b * 32 + 8 * g + 4 * h;
        f32x4 c4 = *(const f32x4*)cp + *(const f32x4*)(cp + 96) + *(const f32x4*)(cp + 192) + *(const f32x4*)(cp + 288);
        float v0 = sc * (acc[b][4 * g] - mean * c4[0]), v1 = sc * (acc[b][4 * g + 1] - mean * c4[1]);
        float v2 = sc * (acc[b][4 * g + 2] - mean * c4[2]), v3 = sc * (acc[b][4 * g + 3] - mean * c4[3]);
        unsigned a = pk2(v0, v1), bb = pk2(v2, v3);
        if (g == 3 && h == 1) { a = 0u; bb = 0u; }
        o[2 * g] = a; o[2 * g + 1] = bb;
        n2 = dot2(a, a, n2); n2 = dot2(bb, bb, n2);
      }
      n2 += xor32(n2);
#pragma unroll
      for (int d = 1; d < 32; d <<= 1) n2 = fmaxf(n2, __shfl_xor(n2, d));
      u16* dst = (type == 0 ? Qf : Kf) + ((size_t)(head * 64 + tb) * 2 * 64 + lane) * 8;
      *(u32x4*)dst = (u32x4){o[0], o[1], o[2], o[3]};
      *(u32x4*)(dst + 512) = (u32x4){o[4], o[5], o[6], o[7]};
      if (lane == 0) (type == 0 ? qn : kn)[head * 64 + tb] = n2;
    }
  }
}

__global__ __launch_bounds__(256) void k_qkv(const u16* __restrict__ x, const u16* __restrict__ w, u16* __restrict__ Qf,
                                             u16* __restrict__ Kf, u16* __restrict__ Vf, float* __restrict__ qn,
                                             float* __restrict__ kn) {
  extern __shared__ __attribute__((aligned(16))) char smem[];
  const int bid = blockIdx.x, xcd = bid & 7, idx = bid >> 3;
  const int mt = xcd * 2 + (idx >> 4), nt = idx & 15;
  if (nt < 10) qkv_body<0>(x, w, Qf, Kf, Vf, qn, kn, smem, mt, nt);
  else if (nt == 10) qkv_body<1>(x, w, Qf, Kf, Vf, qn, kn, smem, mt, nt);
  else qkv_body<3>(x, w, Qf, Kf, Vf, qn, kn, smem, mt, nt);
}

constexpr int B_LDS = 8 * 16384 + 8 * 128 * 4;

template <bool SAFE>
DI void attn_loop(const u32x4* __restrict__ Kp, const u32x4* __restrict__ Vp, const u32x4 (&qf)[4][2],
                  f32x16 (&o)[4], float (&m)[4]) {
  u32x4 kf[2], vf[2], kn_[2], vn_[2];
  kf[0] = Kp[0]; kf[1] = Kp[64]; vf[0] = Vp[0]; vf[1] = Vp[64];
#pragma unroll 1
  for (int kb = 0; kb < 8; ++kb) {
    const int nx = kb < 7 ? kb + 1 : kb;
    kn_[0] = Kp[nx * 128]; kn_[1] = Kp[nx * 128 + 64]; vn_[0] = Vp[nx * 128]; vn_[1] = Vp[nx * 128 + 64];
#pragma unroll
    for (int qb = 0; qb < 4; ++qb) {
      f32x16 s;
#pragma unroll
      for (int i = 0; i < 16; ++i) s[i] = 0.f;
      s = mfma_u(kf[0], qf[qb][0], s);
      s = mfma_u(kf[1], qf[qb][1], s);
      if (!SAFE) {
        float tm = s[0];
#pragma unroll
        for (int i = 1; i < 16; ++i) tm = fmaxf(tm, s[i]);
        tm = fmaxf(tm, xor32(tm));
        const float mn = fmaxf(m[qb], tm);
        const float al = __builtin_amdgcn_exp2f(m[qb] - mn);
        m[qb] = mn;
#pragma unroll
        for (int i = 0; i < 16; ++i) { o[qb][i] *= al; s[i] -= mn; }
      }
      u32x4 p0, p1;
#pragma unroll
      for (int i = 0; i < 4; ++i) {
        p0[i] = pk2(__builtin_amdgcn_exp2f(s[2 * i]), __builtin_amdgcn_exp2f(s[2 * i + 1]));
        p1[i] = pk2(__builtin_amdgcn_exp2f(s[8 + 2 * i]), __builtin_amdgcn_exp2f(s[8 + 2 * i + 1]));
      }
      o[qb] = mfma_u(vf[0], p0, o[qb]);
      o[qb] = mfma_u(vf[1], p1, o[qb]);
    }
    kf[0] = kn_[0]; kf[1] = kn_[1]; vf[0] = vn_[0]; vf[1] = vn_[1];
  }
}

__global__ __launch_bounds__(512) void k_attn(const u16* __restrict__ Qf, const u16* __restrict__ Kf,
                                              const u16* __restrict__ Vf, const float* __restrict__ qn,
                                              const float* __restrict__ kn, u16* __restrict__ O) {
  extern __shared__ __attribute__((aligned(16))) char smem[];
  const int tid = threadIdx.x, lane = tid & 63, wv = __builtin_amdgcn_readfirstlane(tid >> 6);
  const int bid = blockIdx.x, xcd = bid & 7, idx = bid >> 3;
  const int head = xcd * 2 + (idx >> 4), qt = idx & 15;
  u32x4 qf[4][2];
  const u32x4* Qp = (const u32x4*)Qf + (size_t)(head * 64 + qt * 4) * 128 + lane;
#pragma unroll
  for (int qb = 0; qb < 4; ++qb) { qf[qb][0] = Qp[qb * 128]; qf[qb][1] = Qp[qb * 128 + 64]; }
  const u32x4* Kp = (const u32x4*)Kf + (size_t)(head * 64 + wv * 8) * 128 + lane;
  const u32x4* Vp = (const u32x4*)Vf + (size_t)(head * 64 + wv * 8) * 128 + lane;
  float k2 = kn[head * 64 + lane], q2 = qn[head * 64 + qt * 4 + (lane & 3)];
#pragma unroll
  for (int d = 1; d < 64; d <<= 1) { k2 = fmaxf(k2, __shfl_xor(k2, d)); q2 = fmaxf(q2, __shfl_xor(q2, d)); }
  const bool safe = __builtin_amdgcn_readfirstlane((k2 * q2 <= SAFE_T2) ? 1 : 0) != 0;
  f32x16 o[4];
#pragma unroll
  for (int qb = 0; qb < 4; ++qb)
#pragma unroll
    for (int i = 0; i < 16; ++i) o[qb][i] = 0.f;
  float m[4] = {-3.0e38f, -3.0e38f, -3.0e38f, -3.0e38f};
  if (safe) {
    attn_loop<true>(Kp, Vp, qf, o, m);
  } else {
    attn_loop<false>(Kp, Vp, qf, o, m);
    float* ml = (float*)(smem + 8 * 16384);
    if (lane < 32) {
#pragma unroll
      for (int qb = 0; qb < 4; ++qb) ml[wv * 128 + qb * 32 + lane] = m[qb];
    }
    __syncthreads();
#pragma unroll
    for (int qb = 0; qb < 4; ++qb) {
      float M = m[qb];
#pragma unroll
      for (int w2 = 0; w2 < 8; ++w2) M = fmaxf(M, ml[w2 * 128 + qb * 32 + (lane & 31)]);
      const float al = __builtin_amdgcn_exp2f(m[qb] - M);
#pragma unroll
      for (int i = 0; i < 16; ++i) o[qb][i] *= al;
    }
  }
  f32x4* red = (f32x4*)smem;
#pragma unroll
  for (int qb = 0; qb < 4; ++qb)
#pragma unroll
    for (int g = 0; g < 4; ++g)
      red[wv * 1024 + (qb * 4 + g) * 64 + lane] = (f32x4){o[qb][4 * g], o[qb][4 * g + 1], o[qb][4 * g + 2], o[qb][4 * g + 3]};
  __syncthreads();
#pragma unroll
  for (int it = 0; it < 2; ++it) {
    const int item = tid + 512 * it, qg = item >> 6, qb = qg >> 2, g = qg & 3, q = lane & 31, hh = lane >> 5;
    f32x4 a = red[item];
    float l = ((const float*)&red[(qb * 4 + 3) * 64 + 32 + q])[0];
#pragma unroll
    for (int w2 = 1; w2 < 8; ++w2) {
      a += red[w2 * 1024 + item];
      l += ((const float*)&red[w2 * 1024 + (qb * 4 + 3) * 64 + 32 + q])[0];
    }
    const float inv = 1.0f / l;
    if (!(g == 3 && hh == 1)) {
      u32x2 ov = {pk2(a[0] * inv, a[1] * inv), pk2(a[2] * inv, a[3] * inv)};
      *(u32x2*)(O + (size_t)(qt * 128 + qb * 32 + q) * D_ + head * DH_ + 8 * g + 4 * hh) = ov;
    }
  }
}

template <int K, int NBUF>
__global__ __launch_bounds__(256) void k_gemm_res(const u16* __restrict__ A, const u16* __restrict__ B,
                                                  const u16* __restrict__ res, u16* __restrict__ out) {
  extern __shared__ __attribute__((aligned(16))) char smem[];
  constexpr int NS = K / 64, STG = 16384;
  const int tid = threadIdx.x, lane = tid & 63, wv = __builtin_amdgcn_readfirstlane(tid >> 6);
  const int r = lane & 31, h = lane >> 5;
  const int bid = blockIdx.x, xcd = bid & 7, idx = bid >> 3;
  const int mt = xcd * 4 + idx / 7, nt = idx % 7;
  const int m0 = mt * 64, n0 = nt * 64;
  int asrc[2], bsrc[2];
#pragma unroll
  for (int i = 0; i < 2; ++i) {
    int row = 16 * wv + 8 * i + (lane >> 3);
    int c = (lane & 7) ^ ((row >> 1) & 7);
    asrc[i] = ((m0 + row) * K + 8 * c) * 2;
    int cb = (lane & 7) ^ (((row >> 1) & 1) << 2);
    bsrc[i] = (row * D_ + n0 + 8 * cb) * 2;
  }
  auto issue = [&](int s) {
    const unsigned buf = lds_base(smem) + (s % NBUF) * STG;
#pragma unroll
    for (int i = 0; i < 2; ++i) dma16(A + 64 * s, asrc[i], buf + (16 * wv + 8 * i) * 128);
#pragma unroll
    for (int i = 0; i < 2; ++i) dma16(B + 64 * s * D_, bsrc[i], buf + 8192 + (16 * wv + 8 * i) * 128);
  };
  constexpr int PRE = NBUF < NS ? NBUF : NS;
#pragma unroll
  for (int s = 0; s < PRE; ++s) issue(s);
  const int tb = wv & 1, cbk = wv >> 1;
  f32x16 acc;
#pragma unroll
  for (int i = 0; i < 16; ++i) acc[i] = 0.f;
  const int arow = 32 * tb + r, aoff = arow * 128, asw = (arow >> 1) & 7;
  const int q4 = (lane & 15) >> 2, p4 = lane & 3, blk = (lane >> 4) & 1;
  const int bcol = 32 * cbk + 16 * blk + 4 * p4;
  const int boff = 8192 + (8 * h + q4) * 128 + ((((bcol >> 3) ^ (((q4 >> 1) & 1) << 2))) << 4) + (bcol & 7) * 2;
  u32x2 rv[4];
  const u16* rp = res + (size_t)(m0 + 32 * tb + r) * D_ + n0 + 32 * cbk + 4 * h;
#pragma unroll
  for (int s = 0; s < NS; ++s) {
    {
      const int issued = (PRE + (s > 0 ? s - 1 : 0)) < NS ? (PRE + (s > 0 ? s - 1 : 0)) : NS;
      const int out_ = (issued - 1 - s) * 4;
      asm volatile("s_waitcnt vmcnt(%0) lgkmcnt(0)" ::"i"(out_) : "memory");
    }
    BAR();
    if (s >= 1 && s - 1 + PRE < NS) issue(s - 1 + PRE);
    const char* buf = smem + (s % NBUF) * STG;
#pragma unroll
    for (int ks = 0; ks < 4; ++ks) {
      u32x4 af = lds_r128(buf + aoff + (((2 * ks + h) ^ asw) << 4));
      const char* p = buf + boff + ks * 16 * 128;
      u32x4 bf = lds_tr2(p, p + 4 * 128);
      acc = mfma_u(bf, af, acc);
    }
  }
#pragma unroll
  for (int g = 0; g < 4; ++g) rv[g] = *(const u32x2*)(rp + 8 * g);
  u16* op = out + (size_t)(m0 + 32 * tb + r) * D_ + n0 + 32 * cbk + 4 * h;
#pragma unroll
  for (int g = 0; g < 4; ++g) {
    unsigned t0 = pk2(acc[4 * g], acc[4 * g + 1]), t1 = pk2(acc[4 * g + 2], acc[4 * g + 3]);
    u32x2 ov = {pk2(bf_lo(t0) + bf_lo(rv[g][0]), bf_hi(t0) + bf_hi(rv[g][0])),
                pk2(bf_lo(t1) + bf_lo(rv[g][1]), bf_hi(t1) + bf_hi(rv[g][1]))};
    *(u32x2*)(op + 8 * g) = ov;
  }
}

constexpr int D_STG = 49152, D_NBUF = 3, D_MISC = D_STG * D_NBUF;
constexpr int D_LDS = D_MISC + 4 * 128 * 8 + 2 * 256 * 4;

DI float gelu_mul(float g, float u) {
  const float z = g * (1.0f + 0.044715f * g * g) * (-2.0f * 0.7978845608028654f * 1.4426950408889634f);
  const float e = __builtin_amdgcn_exp2f(z);
  return g * u * __builtin_amdgcn_rcpf(1.0f + e);
}

__global__ __launch_bounds__(512) void k_ffn1(const u16* __restrict__ hbuf, const u16* __restrict__ wg,
                                              const u16* __restrict__ wu, u16* __restrict__ act) {
  extern __shared__ __attribute__((aligned(16))) char smem[];
  const int tid = threadIdx.x, lane = tid & 63, wv = __builtin_amdgcn_readfirstlane(tid >> 6);
  const int r = lane & 31, h = lane >> 5;
  const int bid = blockIdx.x, xcd = bid & 7, idx = bid >> 3;
  const int mt = xcd * 2 + idx / 14, nt = idx % 14;
  const int m0 = mt * 128, n0 = nt * 128;
  int asrc[2], bsrc[2];
#pragma unroll
  for (int i = 0; i < 2; ++i) {
    int row = 16 * wv + 8 * i + (lane >> 3);
    int c = (lane & 7) ^ ((row >> 1) & 7);
    asrc[i] = ((m0 + row) * D_ + 8 * c) * 2;
    int brow = 8 * wv + 4 * i + (lane >> 4);
    int cb = (lane & 15) ^ ((brow & 3) << 2);
    bsrc[i] = (brow * F_ + n0 + 8 * cb) * 2;
  }
  auto issue = [&](int s) {
    const unsigned buf = lds_base(smem) + (s % D_NBUF) * D_STG;
#pragma unroll
    for (int i = 0; i < 2; ++i) dma16(hbuf + 64 * s, asrc[i], buf + (2 * wv + i) * 1024);
#pragma unroll
    for (int i = 0; i < 2; ++i) dma16(wg + 64 * s * F_, bsrc[i], buf + 16384 + (2 * wv + i) * 1024);
#pragma unroll
    for (int i = 0; i < 2; ++i) dma16(wu + 64 * s * F_, bsrc[i], buf + 32768 + (2 * wv + i) * 1024);
  };
  issue(0); issue(1); issue(2);
  const int th = wv & 1, fb = wv >> 1;
  f32x16 ag[2], au[2];
#pragma unroll
  for (int t = 0; t < 2; ++t)
#pragma unroll
    for (int i = 0; i < 16; ++i) { ag[t][i] = 0.f; au[t][i] = 0.f; }
  float sum[2] = {0.f, 0.f}, sq[2] = {0.f, 0.f}, csg = 0.f, csu = 0.f;
  const int arow = 64 * th + r, aoff = arow * 128, asw = (arow >> 1) & 7;
  const int q4 = (lane & 15) >> 2, p4 = lane & 3, blk = (lane >> 4) & 1;
  const int bcol = 32 * fb + 16 * blk + 4 * p4;
  const int boff = 16384 + (8 * h + q4) * 256 + (((bcol >> 3) ^ (q4 << 2)) << 4) + (bcol & 7) * 2;
#pragma unroll
  for (int s = 0; s < 7; ++s) {
    if (s == 0) WAIT_V(12);
    else if (s <= 5) WAIT_V(6);
    else WAIT_V(0);
    BAR();
    if (s >= 1 && s + 2 < 7) issue(s + 2);
    const char* buf = smem + (s % D_NBUF) * D_STG;
#pragma unroll
    for (int ks = 0; ks < 4; ++ks) {
      u32x4 af[2];
#pragma unroll
      for (int t = 0; t < 2; ++t) af[t] = lds_r128(buf + aoff + t * 4096 + (((2 * ks + h) ^ asw) << 4));
      const char* p = buf + boff + ks * 16 * 256;
      u32x4 gf = lds_tr2(p, p + 4 * 256);
      u32x4 uf = lds_tr2(p + 16384, p + 16384 + 4 * 256);
#pragma unroll
      for (int t = 0; t < 2; ++t) { ag[t] = mfma_u(gf, af[t], ag[t]); au[t] = mfma_u(uf, af[t], au[t]); }
      if (ks == fb) {
#pragma unroll
        for (int t = 0; t < 2; ++t)
#pragma unroll
          for (int i = 0; i < 4; ++i) { sum[t] = dot2(af[t][i], ONES2, sum[t]); sq[t] = dot2(af[t][i], af[t][i], sq[t]); }
      }
      if ((ks & 1) == th) {
#pragma unroll
        for (int i = 0; i < 4; ++i) { csg = dot2(gf[i], ONES2, csg); csu = dot2(uf[i], ONES2, csu); }
      }
    }
  }
  f32x2* tsp = (f32x2*)(smem + D_MISC);
  float* csp = (float*)(smem + D_MISC + 4 * 128 * 8);
#pragma unroll
  for (int t = 0; t < 2; ++t) { sum[t] += xor32(sum[t]); sq[t] += xor32(sq[t]); }
  csg += xor32(csg); csu += xor32(csu);
  if (h == 0) {
#pragma unroll
    for (int t = 0; t < 2; ++t) tsp[fb * 128 + 64 * th + 32 * t + r] = (f32x2){sum[t], sq[t]};
    csp[th * 256 + 32 * fb + r] = csg;
    csp[th * 256 + 128 + 32 * fb + r] = csu;
  }
  __syncthreads();
  f32x4 cg[4], cu[4];
#pragma unroll
  for (int g = 0; g < 4; ++g) {
    const float* cp = csp + 32 * fb + 8 * g + 4 * h;
    cg[g] = *(const f32x4*)cp + *(const f32x4*)(cp + 256);
    cu[g] = *(const f32x4*)(cp + 128) + *(const f32x4*)(cp + 384);
  }
#pragma unroll
  for (int t = 0; t < 2; ++t) {
    const int tok = 64 * th + 32 * t + r;
    f32x2 st = tsp[tok] + tsp[128 + tok] + tsp[256 + tok] + tsp[384 + tok];
    const float mean = st[0] * (1.0f / D_);
    float var = st[1] * (1.0f / D_) - mean * mean;
    var = var > 0.f ? var : 0.f;
    const float rstd = rsqrtf(var + LN_EPS);
    u16* op = act + (size_t)(m0 + tok) * F_ + n0 + 32 * fb + 4 * h;
#pragma unroll
    for (int g = 0; g < 4; ++g) {
      float v[4];
#pragma unroll
      for (int i = 0; i < 4; ++i) {
        const float gg = rstd * (ag[t][4 * g + i] - mean * cg[g][i]);
        const float uu = rstd * (au[t][4 * g + i] - mean * cu[g][i]);
        v[i] = gelu_mul(gg, uu);
      }
      *(u32x2*)(op + 8 * g) = (u32x2){pk2(v[0], v[1]), pk2(v[2], v[3])};
    }
  }
}

extern "C" void kernel_launch(void* const* d_in, const int* in_sizes, int n_in, void* d_out, int out_size, void* d_ws,
                              size_t ws_size, hipStream_t stream) {
  const u16* x = (const u16*)d_in[0];
  const u16* wqkv = (const u16*)d_in[1];
  const u16* wo = (const u16*)d_in[2];
  const u16* wg = (const u16*)d_in[4];
  const u16* wu = (const u16*)d_in[5];
  const u16* wd = (const u16*)d_in[6];
  u16* out = (u16*)d_out;
  char* ws = (char*)d_ws;
  u16* Qf = (u16*)(ws);
  u16* Kf = (u16*)(ws + (2u << 20));
  u16* Vf = (u16*)(ws + (4u << 20));
  u16* Ob = (u16*)(ws + (6u << 20));
  u16* hb = (u16*)(ws + (8u << 20));
  u16* act = (u16*)(ws + (10u << 20));
  float* qn = (float*)(ws + (18u << 20));
  float* kn = qn + 1024;
  (void)hipFuncSetAttribute((const void*)k_qkv, hipFuncAttributeMaxDynamicSharedMemorySize, A_LDS);
  (void)hipFuncSetAttribute((const void*)k_attn, hipFuncAttributeMaxDynamicSharedMemorySize, B_LDS);
  (void)hipFuncSetAttribute((const void*)k_gemm_res<448, 7>, hipFuncAttributeMaxDynamicSharedMemorySize, 7 * 16384);
  (void)hipFuncSetAttribute((const void*)k_gemm_res<1792, 9>, hipFuncAttributeMaxDynamicSharedMemorySize, 9 * 16384);
  (void)hipFuncSetAttribute((const void*)k_ffn1, hipFuncAttributeMaxDynamicSharedMemorySize, D_LDS);
  k_qkv<<<256, 256, A_LDS, stream>>>(x, wqkv, Qf, Kf, Vf, qn, kn);
  k_attn<<<256, 512, B_LDS, stream>>>(Qf, Kf, Vf, qn, kn, Ob);
  k_gemm_res<448, 7><<<224, 256, 7 * 16384, stream>>>(Ob, wo, x, hb);
  k_ffn1<<<224, 512, D_LDS, stream>>>(hb, wg, wu, act);
  k_gemm_res<1792, 9><<<224, 256, 9 * 16384, stream>>>(act, wd, hb, out);
}
```

```cpp
#include <hip/hip_runtime.h>
#include <hip/hip_bf16.h>

typedef unsigned short u16;
typedef __attribute__((ext_vector_type(8))) short bf16x8;
typedef __attribute__((ext_vector_type(4))) short s16x4;
typedef __attribute__((ext_vector_type(2))) __bf16 b16x2;
typedef __attribute__((ext_vector_type(2))) float f32x2;
typedef __attribute__((ext_vector_type(4))) float f32x4;
typedef __attribute__((ext_vector_type(16))) float f32x16;
typedef __attribute__((ext_vector_type(4))) unsigned u32x4;
typedef __attribute__((ext_vector_type(2))) unsigned u32x2;
#define LAS __attribute__((address_space(3)))
#define DI __device__ __forceinline__
#define WAITV(n) asm volatile("s_waitcnt vmcnt(%0) lgkmcnt(0)" ::"i"(n) : "memory")
#define WAITL() asm volatile("s_waitcnt lgkmcnt(0)" ::: "memory")
#define BAR() __builtin_amdgcn_s_barrier()
#define TSDECL
#define TS(i)
#define TSEND(kid)

constexpr int D_ = 448, DH_ = 28, F_ = 1792, N3_ = 1344;
constexpr float LN_EPS = 1e-5f;
constexpr float QSCALE = 0.18898223650461362f * 1.4426950408889634f;
constexpr float SAFE_T2 = 3600.0f;
constexpr unsigned WS_Q = 0, WS_KV = 2u << 20, WS_O = 6u << 20, WS_H = 8u << 20, WS_ACT = 10u << 20, WS_N = 18u << 20;

DI unsigned pk2(float lo, float hi) {
  f32x2 f = {lo, hi};
  return __builtin_bit_cast(unsigned, __builtin_convertvector(f, b16x2));
}
DI float bf_lo(unsigned u) { return __uint_as_float(u << 16); }
DI float bf_hi(unsigned u) { return __uint_as_float(u & 0xffff0000u); }
DI float rbf(float v) { return bf_lo(pk2(v, v)); }
DI float dot2(unsigned a, unsigned b, float c) {
  return __builtin_amdgcn_fdot2_f32_bf16(__builtin_bit_cast(b16x2, a), __builtin_bit_cast(b16x2, b), c, false);
}
constexpr unsigned ONES2 = 0x3f803f80u;
#define DMA_HEAD "s_mov_b32 %0, m0\n\ts_mov_b32 m0, %1\n\ts_nop 0\n\t"
#define DMA_L(v, o) "global_load_lds_dwordx4 %" #v ", %2 offset:" #o "\n\t"
#define DMA_TAIL "s_mov_b32 m0, %0"
DI void dma2(const void* sb, unsigned lds, int a, int b) {
  unsigned k;
  asm volatile(DMA_HEAD DMA_L(3, 0) DMA_L(4, 1024) DMA_TAIL : "=&s"(k) : "s"(lds), "s"(sb), "v"(a), "v"(b) : "memory");
}
DI void dma3(const void* sb, unsigned lds, int a, int b, int c) {
  unsigned k;
  asm volatile(DMA_HEAD DMA_L(3, 0) DMA_L(4, 1024) DMA_L(5, 2048) DMA_TAIL
               : "=&s"(k) : "s"(lds), "s"(sb), "v"(a), "v"(b), "v"(c) : "memory");
}
DI void dma4(const void* sb, unsigned lds, int a, int b, int c, int d) {
  unsigned k;
  asm volatile(DMA_HEAD DMA_L(3, 0) DMA_L(4, 1024) DMA_L(5, 2048) DMA_L(6, 3072) DMA_TAIL
               : "=&s"(k) : "s"(lds), "s"(sb), "v"(a), "v"(b), "v"(c), "v"(d) : "memory");
}
DI void touch(const void* sb, int voff, unsigned lds) {
  unsigned k;
  asm volatile(DMA_HEAD "global_load_lds_dword %3, %2\n\t" DMA_TAIL : "=&s"(k) : "s"(lds), "s"(sb), "v"(voff) : "memory");
}
#define SB() __builtin_amdgcn_sched_barrier(0)
#define PIN2(a, b) asm volatile("" : "+v"(a), "+v"(b))
#define PIN1(a) asm volatile("" : "+v"(a))
DI unsigned lds_base(char* smem) { return (unsigned)(size_t)(LAS char*)smem; }
DI u32x4 lds_r128(const char* p) { return *(const u32x4*)p; }
DI u32x4 lds_tr2(const char* p0, const char* p1) {
  s16x4 a = __builtin_amdgcn_ds_read_tr16_b64_v4i16((LAS s16x4*)p0);
  s16x4 b = __builtin_amdgcn_ds_read_tr16_b64_v4i16((LAS s16x4*)p1);
  u32x2 ua = __builtin_bit_cast(u32x2, a), ub = __builtin_bit_cast(u32x2, b);
  u32x4 r = {ua[0], ua[1], ub[0], ub[1]};
  return r;
}
DI f32x16 mfma_u(u32x4 a, u32x4 b, f32x16 c) {
  return __builtin_amdgcn_mfma_f32_32x32x16_bf16(__builtin_bit_cast(bf16x8, a), __builtin_bit_cast(bf16x8, b), c, 0, 0, 0);
}
DI f32x2 halves(float v) {
  auto r = __builtin_amdgcn_permlane32_swap(__float_as_uint(v), __float_as_uint(v), false, false);
  return (f32x2){__uint_as_float(r[0]), __uint_as_float(r[1])};
}
DI float sum32(float v) { f32x2 t = halves(v); return t[0] + t[1]; }
DI unsigned wave_max_u(unsigned u) {
  u = max(u, (unsigned)__builtin_amdgcn_update_dpp(0, (int)u, 0x128, 0xf, 0xf, false));
  u = max(u, (unsigned)__builtin_amdgcn_update_dpp(0, (int)u, 0x124, 0xf, 0xf, false));
  u = max(u, (unsigned)__builtin_amdgcn_update_dpp(0, (int)u, 0x122, 0xf, 0xf, false));
  u = max(u, (unsigned)__builtin_amdgcn_update_dpp(0, (int)u, 0x121, 0xf, 0xf, false));
  unsigned a = __builtin_amdgcn_readlane(u, 0), b = __builtin_amdgcn_readlane(u, 16);
  unsigned c = __builtin_amdgcn_readlane(u, 32), d = __builtin_amdgcn_readlane(u, 48);
  return max(max(a, b), max(c, d));
}
DI f32x2 ln_scalars(float sum, float sq) {
  const float mean = sum * (1.0f / D_);
  float var = sq * (1.0f / D_) - mean * mean;
  var = var > 0.f ? var : 0.f;
  const float sd = rbf(sqrtf(rbf(rbf(var) + LN_EPS)));
  return (f32x2){rbf(mean), 1.0f / sd};
}

template <int ND, int E4, int E7>
constexpr int ring_allow(int k, int last) {
  int n = (last - k) * ND;
  if (k <= 4 && last >= 4) n += E4;
  if (k <= 7 && last >= 7) n += E7;
  return n;
}
template <int NS, int NBUF, int STG, int ND, int E4 = 0, int E7 = 0, class F>
DI void ring_loader(F issue) {
#pragma unroll
  for (int s = 0; s < NBUF - 1; ++s) issue(s, s * STG);
  WAITV((NBUF - 2) * ND);
  BAR();
  issue(NBUF - 1, (NBUF - 1) * STG);
#pragma unroll
  for (int s = 0; s < NS - 1; ++s) {
    WAITV((ring_allow<ND, E4, E7>(s + 1, s + NBUF - 1 < NS - 1 ? s + NBUF - 1 : NS - 1)));
    BAR();
    if (s + NBUF < NS) issue(s + NBUF, (s % NBUF) * STG);
  }
}

constexpr int A_XB = 128 * 128, A_WB = 64 * 192, A_STG = A_XB + A_WB, A_NBUF = 4;
constexpr int A_MISC = A_STG * A_NBUF;
constexpr int A_LDS = A_MISC + 5 * 96 * 4 + 128 * 8 + 1024;

template <int GRP, bool ISV>
DI void qkv_body(char* __restrict__ ws, char* smem, int mt, int nt, int wq) {
  TSDECL
  constexpr int NB = GRP == 0 ? 2 : 1, B0 = GRP == 0 ? 0 : 2;
  const int lane = threadIdx.x & 63, r = lane & 31, h = lane >> 5;
  const int off = (84 * nt) & 7;
  f32x16 acc[NB];
  float cs[NB];
#pragma unroll
  for (int b = 0; b < NB; ++b) {
    cs[b] = 0.f;
#pragma unroll
    for (int i = 0; i < 16; ++i) acc[b][i] = 0.f;
  }
  float sum = 0.f, sq = 0.f, sum1 = 0.f, sq1 = 0.f;
  const int xrow = 32 * wq + r, xsw = (xrow >> 1) & 7;
  const int q4 = (lane & 15) >> 2, p4 = lane & 3, blk = (lane >> 4) & 1;
  int ao[4], bo[4];
#pragma unroll
  for (int p = 0; p < 4; ++p) {
    const int kk = (p + wq) & 3;
    ao[p] = xrow * 128 + (((2 * kk + h) ^ xsw) << 4);
    bo[p] = A_XB + (16 * kk + 8 * h + q4) * 192 + (off + 16 * blk + 4 * p4) * 2 + B0 * 56;
  }
  struct Fr { u32x4 a, w[NB]; } f0, f1;
  auto rd = [&](Fr& f, int so, int p) {
    f.a = lds_r128(smem + so + ao[p]);
#pragma unroll
    for (int b = 0; b < NB; ++b) f.w[b] = lds_tr2(smem + so + bo[p] + b * 56, smem + so + bo[p] + b * 56 + 4 * 192);
    SB();
  };
  auto mm = [&](const Fr& f, int p) {
#pragma unroll
    for (int b = 0; b < NB; ++b) acc[b] = ISV ? mfma_u(f.a, f.w[b], acc[b]) : mfma_u(f.w[b], f.a, acc[b]);
    if (GRP == 1) {
#pragma unroll
      for (int i = 0; i < 4; i += 2) {
        sum = dot2(f.a[i], ONES2, sum); sq = dot2(f.a[i], f.a[i], sq);
        sum1 = dot2(f.a[i + 1], ONES2, sum1); sq1 = dot2(f.a[i + 1], f.a[i + 1], sq1);
      }
      PIN2(sum, sq); PIN2(sum1, sq1);
    }
    if (p == 0) {
#pragma unroll
      for (int i = 0; i < 4; ++i)
#pragma unroll
        for (int b = 0; b < NB; ++b) cs[b] = dot2(f.w[b][i], ONES2, cs[b]);
#pragma unroll
      for (int b = 0; b < NB; ++b) PIN1(cs[b]);
    }
    SB();
  };
  BAR();
  TS(2)
  rd(f0, 0, 0);
  int so = 0;
#pragma unroll 1
  for (int s = 0; s < 7; ++s) {
    rd(f1, so, 1); mm(f0, 0);
    rd(f0, so, 2); mm(f1, 1);
    rd(f1, so, 3); mm(f0, 2);
    if (s < 6) {
      WAITL();
      BAR();
      so = so == (A_NBUF - 1) * A_STG ? 0 : so + A_STG;
      rd(f0, so, 0);
    }
    mm(f1, 3);
  }
  TS(9)
  float* csp = (float*)(smem + A_MISC);
  f32x2* tst = (f32x2*)(smem + A_MISC + 5 * 96 * 4);
#pragma unroll
  for (int b = 0; b < NB; ++b) {
    const float c = sum32(cs[b]);
    if (h == 0) csp[wq * 96 + (B0 + b) * 32 + r] = c;
  }
  if (GRP == 1) {
    const f32x2 st = ln_scalars(sum32(sum + sum1), sum32(sq + sq1));
    if (h == 0) tst[32 * wq + r] = st;
  }
  WAITL();
  BAR();
  BAR();
  const float* ct = csp + 384;
  const int tb = mt * 4 + wq;
#pragma unroll
  for (int b = 0; b < NB; ++b) {
    const int gb = 3 * nt + B0 + b, head = gb & 15, type = gb >> 4, cb = (B0 + b) * 32;
    unsigned o[8];
    if (ISV) {
      const float c = ct[cb + r];
#pragma unroll
      for (int g = 0; g < 4; ++g) {
        const float* sp = (const float*)(tst + 32 * wq + 8 * g + 4 * h);
        f32x4 s0 = *(const f32x4*)sp, s1 = *(const f32x4*)(sp + 4);
        float v0 = s0[1] * (acc[b][4 * g] - s0[0] * c), v1 = s0[3] * (acc[b][4 * g + 1] - s0[2] * c);
        float v2 = s1[1] * (acc[b][4 * g + 2] - s1[0] * c), v3 = s1[3] * (acc[b][4 * g + 3] - s1[2] * c);
        o[2 * g] = pk2(v0, v1); o[2 * g + 1] = pk2(v2, v3);
      }
      if (r >= DH_) {
        const unsigned f = (r == DH_) ? ONES2 : 0u;
#pragma unroll
        for (int i = 0; i < 8; ++i) o[i] = f;
      }
      char* dst = ws + WS_KV + (head * 64 + tb) * 4096 + 2048 + lane * 16;
      *(u32x4*)dst = (u32x4){o[0], o[1], o[2], o[3]};
      *(u32x4*)(dst + 1024) = (u32x4){o[4], o[5], o[6], o[7]};
    } else {
      const f32x2 st = tst[32 * wq + r];
      const float sc = type == 0 ? st[1] * QSCALE : st[1];
      float n2 = 0.f;
#pragma unroll
      for (int g = 0; g < 4; ++g) {
        const f32x4 c4 = *(const f32x4*)(ct + cb + 8 * g + 4 * h);
        float v0 = sc * (acc[b][4 * g] - st[0] * c4[0]), v1 = sc * (acc[b][4 * g + 1] - st[0] * c4[1]);
        float v2 = sc * (acc[b][4 * g + 2] - st[0] * c4[2]), v3 = sc * (acc[b][4 * g + 3] - st[0] * c4[3]);
        unsigned a = pk2(v0, v1), bb = pk2(v2, v3);
        if (g == 3 && h == 1) { a = 0u; bb = 0u; }
        o[2 * g] = a; o[2 * g + 1] = bb;
        n2 = dot2(a, a, n2); n2 = dot2(bb, bb, n2);
      }
      char* dst = ws + (type == 0 ? WS_Q + (head * 64 + tb) * 2048 : WS_KV + (head * 64 + tb) * 4096) + lane * 16;
      *(u32x4*)dst = (u32x4){o[0], o[1], o[2], o[3]};
      *(u32x4*)(dst + 1024) = (u32x4){o[4], o[5], o[6], o[7]};
      const unsigned mx = wave_max_u(__float_as_uint(sum32(n2)));
      if (lane == 0) ((unsigned*)(ws + WS_N))[type * 1024 + head * 64 + tb] = mx;
    }
  }
  TSEND(0)
}

__global__ __launch_bounds__(768) void k_qkv(const u16* __restrict__ x, const u16* __restrict__ w, char* __restrict__ ws) {
  extern __shared__ __attribute__((aligned(16))) char smem[];
  const int bid = blockIdx.x, xcd = bid & 7, idx = bid >> 3;
  const int mt = (xcd >> 1) * 4 + (idx >> 3), nt = (xcd & 1) * 8 + (idx & 7);
  const int wv = __builtin_amdgcn_readfirstlane(threadIdx.x >> 6), wq = wv & 3;
  if (wv >= 8) {
    const int lane = threadIdx.x & 63, t = wq * 64 + lane;
    {
      const unsigned junk = lds_base(smem) + A_LDS - 1024 + wq * 256;
      const int t1 = t < 112 ? t : 111, t2 = t < 154 ? t : 153;
      touch(w, (idx * 14 + t2 / 11) * (N3_ * 2) + ((xcd & 1) * 10 + t2 % 11) * 128, junk);
      touch(x, ((xcd >> 1) * 512 + idx * 16) * (D_ * 2) + t1 * 128, junk);
    }
    const int ca = (84 * nt) & ~7;
    int sw[3], sx[4];
#pragma unroll
    for (int i = 0; i < 3; ++i) {
      int ci = 64 * (3 * wq + i) + lane, row = ci / 12, c = ci - row * 12;
      int col = ca + 8 * c;
      col = col > (N3_ - 8) ? (N3_ - 8) : col;
      sw[i] = (row * N3_ + col) * 2 - 1024 * i;
    }
#pragma unroll
    for (int i = 0; i < 4; ++i) {
      int c = (lane & 7) ^ (((i & 1) * 4 + (lane >> 4)) & 7);
      sx[i] = ((mt * 128 + 32 * wq + 8 * i + (lane >> 3)) * D_ + 8 * c) * 2 - 1024 * i;
    }
    ring_loader<7, A_NBUF, A_STG, 7>([&](int s, int so) {
      dma3(w + 64 * s * N3_, lds_base(smem) + so + A_XB + 3 * wq * 1024, sw[0], sw[1], sw[2]);
      dma4(x + 64 * s, lds_base(smem) + so + wq * 4096, sx[0], sx[1], sx[2], sx[3]);
    });
    BAR();
    float* csp = (float*)(smem + A_MISC);
    if (t < 96) csp[384 + t] = csp[t] + csp[96 + t] + csp[192 + t] + csp[288 + t];
    WAITL();
    BAR();
  } else if (wv < 4) {
    if (nt >= 11) qkv_body<0, true>(ws, smem, mt, nt, wq);
    else qkv_body<0, false>(ws, smem, mt, nt, wq);
  } else {
    if (nt >= 10) qkv_body<1, true>(ws, smem, mt, nt, wq);
    else qkv_body<1, false>(ws, smem, mt, nt, wq);
  }
}

constexpr int B_LDS = 4 * 32768 + 1024;

template <bool SAFE>
DI void attn_loop(char* smem, const u32x4 (&qf)[2], f32x16& o, float& m, int kh) {
  const int lane = threadIdx.x & 63;
  const f32x16 zero = {};
  BAR();
  const char* lp = smem + kh * 16384 + lane * 16;
  u32x4 kk[2][2], vv[4][2];
  f32x16 sc[2];
  kk[0][0] = lds_r128(lp); kk[0][1] = lds_r128(lp + 1024);
  kk[1][0] = lds_r128(lp + 4096); kk[1][1] = lds_r128(lp + 5120);
  vv[0][0] = lds_r128(lp + 2048); vv[0][1] = lds_r128(lp + 3072);
  vv[1][0] = lds_r128(lp + 6144); vv[1][1] = lds_r128(lp + 7168);
  sc[0] = mfma_u(kk[0][1], qf[1], mfma_u(kk[0][0], qf[0], zero));
  SB();
#pragma unroll 1
  for (int s = 0; s < 8; ++s) {
#pragma unroll
    for (int b = 0; b < 4; ++b) {
      f32x16& cur = sc[b & 1];
      f32x16& nxt = sc[(b + 1) & 1];
      nxt = mfma_u(kk[(b + 1) & 1][0], qf[0], zero);
      SB();
      if (b == 2 && s < 7) {
        WAITL();
        BAR();
      }
      const char* np = lp + (b >= 2 ? ((s + 1) & 3) * 32768 + (b - 2) * 4096 : (s & 3) * 32768 + (b + 2) * 4096);
      kk[b & 1][0] = lds_r128(np); kk[b & 1][1] = lds_r128(np + 1024);
      vv[(b + 2) & 3][0] = lds_r128(np + 2048); vv[(b + 2) & 3][1] = lds_r128(np + 3072);
      SB();
      if (!SAFE) {
        float tm = cur[0];
#pragma unroll
        for (int i = 1; i < 16; ++i) tm = fmaxf(tm, cur[i]);
        const f32x2 t2 = halves(tm);
        const float mn = fmaxf(m, fmaxf(t2[0], t2[1]));
        const float al = __builtin_amdgcn_exp2f(m - mn);
        m = mn;
#pragma unroll
        for (int i = 0; i < 16; ++i) { o[i] *= al; cur[i] -= mn; }
      }
      u32x4 p0, p1;
#pragma unroll
      for (int i = 0; i < 4; ++i) p0[i] = pk2(__builtin_amdgcn_exp2f(cur[2 * i]), __builtin_amdgcn_exp2f(cur[2 * i + 1]));
      SB();
      nxt = mfma_u(kk[(b + 1) & 1][1], qf[1], nxt);
      o = mfma_u(vv[b][0], p0, o);
      SB();
#pragma unroll
      for (int i = 0; i < 4; ++i)
        p1[i] = pk2(__builtin_amdgcn_exp2f(cur[8 + 2 * i]), __builtin_amdgcn_exp2f(cur[8 + 2 * i + 1]));
      SB();
      o = mfma_u(vv[b][1], p1, o);
      SB();
    }
  }
}

__global__ __launch_bounds__(768) void k_attn(char* __restrict__ ws, const u16* __restrict__ wo, const u16* __restrict__ wg,
                                              const u16* __restrict__ wu, const u16* __restrict__ wd) {
  extern __shared__ __attribute__((aligned(16))) char smem[];
  TSDECL
  const int lane = threadIdx.x & 63, wv = __builtin_amdgcn_readfirstlane(threadIdx.x >> 6);
  const int bid = blockIdx.x, xcd = bid & 7, idx = bid >> 3;
  const int head = xcd * 2 + (idx >> 4), qt = idx & 15, qb = wv & 3, kh = wv >> 2, tb = qt * 4 + qb;
  if (wv >= 8) {
    const char* kv = ws + WS_KV + head * (64 * 4096);
    const int t = qb * 64 + lane, l16 = lane * 16;
    const unsigned junk = lds_base(smem) + 4 * 32768 + qb * 256;
    touch(kv, (qt * 128 + (t & 127)) * 128, junk);
    const int t1 = t < 98 ? t : 97, t2 = t < 136 ? t + 256 : t;
    const int go = (idx * 14 + t1 / 7) * (F_ * 2) + (xcd & 3) * 896 + (t1 % 7) * 128;
    ring_loader<8, 4, 32768, 8, 3, 2>([&](int s, int so) {
      const unsigned dst = lds_base(smem) + so + qb * 8192;
      const char* src = kv + ((qb >> 1) * 32 + s * 4) * 4096 + (qb & 1) * 8192;
      dma4(src, dst, l16, l16, l16, l16);
      dma4(src + 4096, dst + 4096, l16, l16, l16, l16);
      if (s == 4) { touch(wo, (idx * 98 + t1) * 128, junk); touch(wg, go, junk); touch(wu, go, junk); }
      if (s == 7) { touch(wd, (idx * 392 + t) * 128, junk); touch(wd, (idx * 392 + t2) * 128, junk); }
    });
    WAITV(0);
    return;
  }
  const char* qp = ws + WS_Q + (head * 64 + tb) * 2048 + lane * 16;
  const u32x4 qf[2] = {*(const u32x4*)qp, *(const u32x4*)(qp + 1024)};
  const unsigned* nn = (const unsigned*)(ws + WS_N) + head * 64 + lane;
  const unsigned nq = nn[0], k2 = wave_max_u(nn[1024]), q2 = wave_max_u((lane >> 2) == qt ? nq : 0u);
  const bool safe = __uint_as_float(k2) * __uint_as_float(q2) <= SAFE_T2;
  f32x16 o = {};
  float m = -3.0e38f;
  TS(2)
  if (safe) attn_loop<true>(smem, qf, o, m, kh);
  else attn_loop<false>(smem, qf, o, m, kh);
  TS(3)
  f32x4* red = (f32x4*)(smem + 32768) + qb * 320 + lane;
  if (kh == 1) {
#pragma unroll
    for (int g = 0; g < 4; ++g) red[g * 64] = (f32x4){o[4 * g], o[4 * g + 1], o[4 * g + 2], o[4 * g + 3]};
    ((float*)(red - lane + 256))[lane] = m;
  }
  WAITL();
  BAR();
  if (kh == 0) {
    float a0 = 1.f, a1 = 1.f;
    if (!safe) {
      const float m1 = ((const float*)(red - lane + 256))[lane], mm = fmaxf(m, m1);
      a0 = __builtin_amdgcn_exp2f(m - mm); a1 = __builtin_amdgcn_exp2f(m1 - mm);
    }
#pragma unroll
    for (int g = 0; g < 4; ++g) {
      const f32x4 t = red[g * 64];
#pragma unroll
      for (int i = 0; i < 4; ++i) o[4 * g + i] = o[4 * g + i] * a0 + t[i] * a1;
    }
    const float inv = 1.0f / halves(o[12])[1];
    unsigned u[8];
#pragma unroll
    for (int i = 0; i < 8; ++i) u[i] = pk2(o[2 * i] * inv, o[2 * i + 1] * inv);
    if (lane >= 32) { u[6] = 0u; u[7] = 0u; }
    char* dst = ws + WS_O + (tb * 16 + head) * 2048 + lane * 16;
    *(u32x4*)dst = (u32x4){u[0], u[1], u[2], u[3]};
    *(u32x4*)(dst + 1024) = (u32x4){u[4], u[5], u[6], u[7]};
  }
  TSEND(1)
}

constexpr int C_LDS = 4 * 32768 + 1024;
template <int MODE>
__global__ __launch_bounds__(768) void k_gemm_res(char* __restrict__ ws, const u16* __restrict__ B, const u16* __restrict__ x,
                                                  u16* __restrict__ out) {
  extern __shared__ __attribute__((aligned(16))) char smem[];
  TSDECL
  constexpr int NS = MODE ? 14 : 4, NBUF = 4, STG = 32768, KS = MODE ? 112 : 32, RPS = MODE ? 128 : 112;
  const int lane = threadIdx.x & 63, wv = __builtin_amdgcn_readfirstlane(threadIdx.x >> 6);
  const int r = lane & 31, h = lane >> 5;
  const int bid = blockIdx.x, xcd = bid & 7, idx = bid >> 3;
  const int mt = xcd * 4 + idx / 7, nt = idx % 7;
  if (wv >= 8) {
    const int lw = wv - 8, t = lw * 64 + lane, l16 = lane * 16;
    const char* A0 = ws + (MODE ? WS_ACT : WS_O) + (size_t)mt * 2 * KS * 1024;
    constexpr int TL = MODE ? 256 : 74, TOT = 2 * KS * 8;
    int tl = nt * TL + (t < TL ? t : 0);
    tl = tl < TOT ? tl : TOT - 1;
    touch(A0, tl * 128, lds_base(smem) + 4 * 32768 + lw * 256);
    const char* A = A0 + (lw >> 1) * KS * 1024 + (lw & 1) * 4096;
    int bs[4];
#pragma unroll
    for (int i = 0; i < 4; ++i) {
      int row = 32 * lw + 8 * i + (lane >> 3);
      const int cb = (lane & 7) ^ (((row >> 1) & 1) << 2);
      if (MODE == 0 && row >= 112) row -= 16;
      bs[i] = (row * D_ + nt * 64 + 8 * cb) * 2 - 1024 * i;
    }
    ring_loader<NS, NBUF, STG, 8>([&](int s, int so) {
      const unsigned buf = lds_base(smem) + so + lw * 4096;
      dma4(B + s * RPS * D_, buf + 16384, bs[0], bs[1], bs[2], bs[3]);
      dma4(A + s * 8192, buf, l16, l16, l16, l16);
    });
    return;
  }
  const int tbl = wv & 1, cbk = (wv >> 1) & 1, kh = wv >> 2;
  u32x4 rv[2];
  char* hp = ws + WS_H + ((mt * 2 + tbl) * 28 + nt * 4 + cbk * 2) * 1024 + lane * 16;
  if (kh == 0) {
    if (MODE) { rv[0] = *(const u32x4*)hp; rv[1] = *(const u32x4*)(hp + 1024); }
    else {
      const u16* rp = x + (size_t)(mt * 64 + tbl * 32 + r) * D_ + nt * 64 + cbk * 32 + 4 * h;
#pragma unroll
      for (int g = 0; g < 4; ++g) { u32x2 t = *(const u32x2*)(rp + 8 * g); rv[g >> 1][2 * (g & 1)] = t[0]; rv[g >> 1][2 * (g & 1) + 1] = t[1]; }
    }
  }
  f32x16 acc = {};
  const int q4 = (lane & 15) >> 2, p4 = lane & 3, blk = (lane >> 4) & 1;
  const int bcol = 32 * cbk + 16 * blk + 4 * p4;
  const int aoff = tbl * 8192 + kh * 4096 + lane * 16;
  const int boff = 16384 + ((MODE ? 64 : 56) * kh + 4 * h + q4) * 128 + (((bcol >> 3) ^ ((q4 >> 1) << 2)) << 4) + (bcol & 7) * 2;
  struct Fr { u32x4 a[4], w[4]; } f0, f1;
  auto rd = [&](Fr& f, int so) {
#pragma unroll
    for (int ks = 0; ks < 4; ++ks) {
      f.a[ks] = lds_r128(smem + so + aoff + ks * 1024);
      const char* p = smem + so + boff + (MODE ? 16 * ks : 28 * (ks >> 1) + 16 * (ks & 1)) * 128;
      f.w[ks] = lds_tr2(p, p + 8 * 128);
    }
    SB();
  };
  auto mm = [&](const Fr& f) {
#pragma unroll
    for (int ks = 0; ks < 4; ++ks) acc = mfma_u(f.w[ks], f.a[ks], acc);
    SB();
  };
  BAR();
  TS(2)
  rd(f0, 0);
#pragma unroll 1
  for (int s = 0; s < NS; s += 2) {
    WAITL();
    BAR();
    rd(f1, ((s + 1) & 3) * STG); mm(f0);
    if (s + 2 < NS) { WAITL(); BAR(); rd(f0, ((s + 2) & 3) * STG); }
    mm(f1);
  }
  TS(9)
  char* fr = smem + (NS % NBUF) * STG;
  f32x4* red = (f32x4*)fr + (wv & 3) * 256 + lane;
  if (kh == 1) {
#pragma unroll
    for (int g = 0; g < 4; ++g) red[g * 64] = (f32x4){acc[4 * g], acc[4 * g + 1], acc[4 * g + 2], acc[4 * g + 3]};
  }
  WAITL();
  BAR();
  if (kh == 0) {
    unsigned u[8];
#pragma unroll
    for (int g = 0; g < 4; ++g) {
      const f32x4 t = red[g * 64];
      const unsigned t0 = pk2(acc[4 * g] + t[0], acc[4 * g + 1] + t[1]), t1 = pk2(acc[4 * g + 2] + t[2], acc[4 * g + 3] + t[3]);
      const unsigned r0 = rv[g >> 1][2 * (g & 1)], r1 = rv[g >> 1][2 * (g & 1) + 1];
      u[2 * g] = pk2(bf_lo(t0) + bf_lo(r0), bf_hi(t0) + bf_hi(r0));
      u[2 * g + 1] = pk2(bf_lo(t1) + bf_lo(r1), bf_hi(t1) + bf_hi(r1));
    }
    if (MODE == 0) {
      *(u32x4*)hp = (u32x4){u[0], u[1], u[2], u[3]};
      *(u32x4*)(hp + 1024) = (u32x4){u[4], u[5], u[6], u[7]};
    } else {
      char* tp = fr + 16384 + (tbl * 32 + r) * 136 + cbk * 64 + h * 8;
#pragma unroll
      for (int g = 0; g < 4; ++g) *(u32x2*)(tp + 16 * g) = (u32x2){u[2 * g], u[2 * g + 1]};
    }
  }
  if (MODE == 1) {
    WAITL();
    BAR();
    const int row = wv * 8 + (lane >> 3);
    const char* tp = fr + 16384 + row * 136 + (lane & 7) * 16;
    const u32x2 a = *(const u32x2*)tp, b = *(const u32x2*)(tp + 8);
    *(u32x4*)(out + (size_t)(mt * 64 + row) * D_ + nt * 64 + (lane & 7) * 8) = (u32x4){a[0], a[1], b[0], b[1]};
  }
  TSEND(2 + 2 * MODE)
}

constexpr int D_U = 64 * 224 + 128, D_STG = 16384 + D_U + 64 * 224, D_NBUF = 3, D_MISC = D_STG * D_NBUF;
constexpr int D_LDS = D_MISC + 5 * 224 * 4 + 128 * 8 + 1024;

template <int GRP>
DI void ffn1_body(char* __restrict__ ws, char* smem, int mt, int nt, int wq) {
  TSDECL
  constexpr int NB = GRP == 0 ? 4 : 3, B0 = GRP == 0 ? 0 : 4;
  const int lane = threadIdx.x & 63, r = lane & 31, h = lane >> 5;
  f32x16 acc[NB];
  float cs[NB];
#pragma unroll
  for (int b = 0; b < NB; ++b) {
    cs[b] = 0.f;
#pragma unroll
    for (int i = 0; i < 16; ++i) acc[b][i] = 0.f;
  }
  float sum = 0.f, sq = 0.f, sum1 = 0.f, sq1 = 0.f;
  const int q4 = (lane & 15) >> 2, p4 = lane & 3, blk = (lane >> 4) & 1;
  int ao[4], bo[4];
#pragma unroll
  for (int p = 0; p < 4; ++p) {
    const int kk = (p + wq) & 3;
    ao[p] = wq * 4096 + kk * 1024 + lane * 16;
    bo[p] = 16384 + blk * D_U + (16 * kk + 4 * h + q4) * 224 + 32 * B0 + 8 * p4;
  }
  struct Fr { u32x4 a, w[NB]; } f0, f1;
  auto rd = [&](Fr& f, int so, int p) {
    f.a = lds_r128(smem + so + ao[p]);
#pragma unroll
    for (int b = 0; b < NB; ++b) f.w[b] = lds_tr2(smem + so + bo[p] + b * 32, smem + so + bo[p] + b * 32 + 8 * 224);
    SB();
  };
  auto mm = [&](const Fr& f, int p) {
#pragma unroll
    for (int b = 0; b < NB; ++b) acc[b] = mfma_u(f.w[b], f.a, acc[b]);
    if (GRP == 1) {
#pragma unroll
      for (int i = 0; i < 4; i += 2) {
        sum = dot2(f.a[i], ONES2, sum); sq = dot2(f.a[i], f.a[i], sq);
        sum1 = dot2(f.a[i + 1], ONES2, sum1); sq1 = dot2(f.a[i + 1], f.a[i + 1], sq1);
      }
      PIN2(sum, sq); PIN2(sum1, sq1);
    }
    if (p == 0) {
#pragma unroll
      for (int i = 0; i < 4; ++i)
#pragma unroll
        for (int b = 0; b < NB; ++b) cs[b] = dot2(f.w[b][i], ONES2, cs[b]);
#pragma unroll
      for (int b = 0; b < NB; ++b) PIN1(cs[b]);
    }
    SB();
  };
  BAR();
  TS(2)
  rd(f0, 0, 0);
  int so = 0;
#pragma unroll 1
  for (int s = 0; s < 7; ++s) {
    rd(f1, so, 1); mm(f0, 0);
    rd(f0, so, 2); mm(f1, 1);
    rd(f1, so, 3); mm(f0, 2);
    if (s < 6) {
      WAITL();
      BAR();
      so = so == 2 * D_STG ? 0 : so + D_STG;
      rd(f0, so, 0);
    }
    mm(f1, 3);
  }
  TS(9)
  float* csp = (float*)(smem + D_MISC);
  f32x2* tst = (f32x2*)(smem + D_MISC + 5 * 224 * 4);
#pragma unroll
  for (int b = 0; b < NB; ++b) {
    const float c = sum32(cs[b]);
    if (h == 0) csp[wq * 224 + (B0 + b) * 32 + r] = c;
  }
  if (GRP == 1) {
    const f32x2 st = ln_scalars(sum32(sum + sum1), sum32(sq + sq1));
    if (h == 0) tst[32 * wq + r] = st;
  }
  WAITL();
  BAR();
  BAR();
  const f32x2 st = tst[32 * wq + r];
  const float c1 = st[1] * (-2.0f * 0.7978845608028654f * 1.4426950408889634f);
  const float c3 = c1 * st[1] * st[1] * 0.044715f, ir2 = 1.0f / (st[1] * st[1]), nm = -st[0];
  char* dst = ws + WS_ACT + (size_t)((mt * 4 + wq) * 112 + nt * 7 + B0) * 1024 + lane * 16;
#pragma unroll
  for (int b = 0; b < NB; ++b) {
    float y[16];
#pragma unroll
    for (int g = 0; g < 4; ++g) {
      const f32x4 c4 = *(const f32x4*)(csp + 896 + (B0 + b) * 32 + 8 * g + 4 * h);
#pragma unroll
      for (int i = 0; i < 4; ++i) y[4 * g + i] = __builtin_fmaf(nm, c4[i], acc[b][4 * g + i]);
    }
    float v[8];
#pragma unroll
    for (int i = 0; i < 8; ++i) {
      const float e = __builtin_amdgcn_exp2f(__builtin_fmaf(y[i] * y[i], c3, c1) * y[i]);
      v[i] = y[i] * y[8 + i] * __builtin_amdgcn_rcpf(__builtin_fmaf(e, ir2, ir2));
    }
    *(u32x4*)(dst + b * 1024) = (u32x4){pk2(v[0], v[1]), pk2(v[2], v[3]), pk2(v[4], v[5]), pk2(v[6], v[7])};
  }
  TSEND(3)
}

__global__ __launch_bounds__(768) void k_ffn1(char* __restrict__ ws, const u16* __restrict__ wg, const u16* __restrict__ wu) {
  extern __shared__ __attribute__((aligned(16))) char smem[];
  const int wv = __builtin_amdgcn_readfirstlane(threadIdx.x >> 6), wq = wv & 3;
  const int bid = blockIdx.x, xcd = bid & 7, idx = bid >> 3;
  const int mt = (xcd >> 2) * 8 + (idx >> 2), nt = (xcd & 3) * 4 + (idx & 3);
  if (wv >= 8) {
    const int lane = threadIdx.x & 63, t = wq * 64 + lane, l16 = lane * 16;
    const char* hsrc = ws + WS_H + mt * 4 * 28 * 1024;
    touch(hsrc, ((idx & 3) * 224 + (t < 224 ? t : 0)) * 128, lds_base(smem) + D_LDS - 1024 + wq * 256);
    hsrc += wq * 28 * 1024;
    int src[7];
#pragma unroll
    for (int i = 0; i < 7; ++i) {
      const int c = (wq & 1) * 448 + i * 64 + lane, row = c / 14, j = c - row * 14;
      src[i] = row * (F_ * 2) + nt * 224 + j * 16 - 1024 * (i & 3);
    }
    const char* wsrc = (const char*)(wq < 2 ? wg : wu);
    ring_loader<7, D_NBUF, D_STG, 11>([&](int s, int so) {
      const unsigned buf = lds_base(smem) + so + 16384 + (wq >> 1) * D_U + (wq & 1) * 7168;
      dma4(wsrc + s * (64 * F_ * 2), buf, src[0], src[1], src[2], src[3]);
      dma3(wsrc + s * (64 * F_ * 2), buf + 4096, src[4], src[5], src[6]);
      dma4(hsrc + s * 4096, lds_base(smem) + so + wq * 4096, l16, l16, l16, l16);
    });
    BAR();
    float* csp = (float*)(smem + D_MISC);
    if (t < 224) csp[896 + t] = csp[t] + csp[224 + t] + csp[448 + t] + csp[672 + t];
    WAITL();
    BAR();
  } else if (wv < 4) ffn1_body<0>(ws, smem, mt, nt, wq);
  else ffn1_body<1>(ws, smem, mt, nt, wq);
}

extern "C" void kernel_launch(void* const* d_in, const int* in_sizes, int n_in, void* d_out, int out_size, void* d_ws,
                              size_t ws_size, hipStream_t stream) {
  const u16* x = (const u16*)d_in[0];
  const u16* wqkv = (const u16*)d_in[1];
  const u16* wo = (const u16*)d_in[2];
  const u16* wg = (const u16*)d_in[4];
  const u16* wu = (const u16*)d_in[5];
  const u16* wd = (const u16*)d_in[6];
  u16* out = (u16*)d_out;
  char* ws = (char*)d_ws;
  (void)hipFuncSetAttribute((const void*)k_qkv, hipFuncAttributeMaxDynamicSharedMemorySize, A_LDS);
  (void)hipFuncSetAttribute((const void*)k_attn, hipFuncAttributeMaxDynamicSharedMemorySize, B_LDS);
  (void)hipFuncSetAttribute((const void*)k_gemm_res<0>, hipFuncAttributeMaxDynamicSharedMemorySize, C_LDS);
  (void)hipFuncSetAttribute((const void*)k_gemm_res<1>, hipFuncAttributeMaxDynamicSharedMemorySize, C_LDS);
  (void)hipFuncSetAttribute((const void*)k_ffn1, hipFuncAttributeMaxDynamicSharedMemorySize, D_LDS);
  k_qkv<<<256, 768, A_LDS, stream>>>(x, wqkv, ws);
  k_attn<<<256, 768, B_LDS, stream>>>(ws, wo, wg, wu, wd);
  k_gemm_res<0><<<224, 768, C_LDS, stream>>>(ws, wo, x, out);
  k_ffn1<<<256, 768, D_LDS, stream>>>(ws, wg, wu);
  k_gemm_res<1><<<224, 768, C_LDS, stream>>>(ws, wd, x, out);
}
```

```cpp
#include <hip/hip_runtime.h>
#include <hip/hip_bf16.h>

typedef unsigned short u16;
typedef __attribute__((ext_vector_type(8))) short bf16x8;
typedef __attribute__((ext_vector_type(4))) short s16x4;
typedef __attribute__((ext_vector_type(2))) __bf16 b16x2;
typedef __attribute__((ext_vector_type(2))) float f32x2;
typedef __attribute__((ext_vector_type(4))) float f32x4;
typedef __attribute__((ext_vector_type(16))) float f32x16;
typedef __attribute__((ext_vector_type(4))) unsigned u32x4;
typedef __attribute__((ext_vector_type(2))) unsigned u32x2;
#define LAS __attribute__((address_space(3)))
#define DI __device__ __forceinline__
#define WAITV(n) asm volatile("s_waitcnt vmcnt(%0) lgkmcnt(0)" ::"i"(n) : "memory")
#define WAITL() asm volatile("s_waitcnt lgkmcnt(0)" ::: "memory")
#define BAR() __builtin_amdgcn_s_barrier()
#define TSDECL
#define TS(i)
#define TSEND(kid)

constexpr int D_ = 448, DH_ = 28, F_ = 1792, N3_ = 1344;
constexpr float LN_EPS = 1e-5f;
constexpr float QSCALE = 0.18898223650461362f * 1.4426950408889634f;
constexpr float SAFE_T2 = 3600.0f;
constexpr unsigned WS_Q = 0, WS_KV = 2u << 20, WS_O = 6u << 20, WS_H = 8u << 20, WS_ACT = 10u << 20, WS_N = 18u << 20, WS_ST = 19u << 20, WS_CS = 20u << 20;

DI unsigned pk2(float lo, float hi) {
  f32x2 f = {lo, hi};
  return __builtin_bit_cast(unsigned, __builtin_convertvector(f, b16x2));
}
DI float bf_lo(unsigned u) { return __uint_as_float(u << 16); }
DI float bf_hi(unsigned u) { return __uint_as_float(u & 0xffff0000u); }
DI float rbf(float v) { return bf_lo(pk2(v, v)); }
DI float dot2(unsigned a, unsigned b, float c) {
  return __builtin_amdgcn_fdot2_f32_bf16(__builtin_bit_cast(b16x2, a), __builtin_bit_cast(b16x2, b), c, false);
}
DI float fma1(float a, float b, float c) {
  float d;
  asm("v_fma_f32 %0, %1, %2, %3" : "=v"(d) : "v"(a), "v"(b), "v"(c));
  return d;
}
constexpr unsigned ONES2 = 0x3f803f80u;
#define DMA_HEAD "s_mov_b32 %0, m0\n\ts_mov_b32 m0, %1\n\ts_nop 0\n\t"
#define DMA_L(v, o) "global_load_lds_dwordx4 %" #v ", %2 offset:" #o "\n\t"
#define DMA_TAIL "s_mov_b32 m0, %0"
DI void dma2(const void* sb, unsigned lds, int a, int b) {
  unsigned k;
  asm volatile(DMA_HEAD DMA_L(3, 0) DMA_L(4, 1024) DMA_TAIL : "=&s"(k) : "s"(lds), "s"(sb), "v"(a), "v"(b) : "memory");
}
DI void dma3(const void* sb, unsigned lds, int a, int b, int c) {
  unsigned k;
  asm volatile(DMA_HEAD DMA_L(3, 0) DMA_L(4, 1024) DMA_L(5, 2048) DMA_TAIL
               : "=&s"(k) : "s"(lds), "s"(sb), "v"(a), "v"(b), "v"(c) : "memory");
}
DI void dma4(const void* sb, unsigned lds, int a, int b, int c, int d) {
  unsigned k;
  asm volatile(DMA_HEAD DMA_L(3, 0) DMA_L(4, 1024) DMA_L(5, 2048) DMA_L(6, 3072) DMA_TAIL
               : "=&s"(k) : "s"(lds), "s"(sb), "v"(a), "v"(b), "v"(c), "v"(d) : "memory");
}
DI void touch(const void* sb, int voff, unsigned lds) {
  unsigned k;
  asm volatile(DMA_HEAD "global_load_lds_dword %3, %2\n\t" DMA_TAIL : "=&s"(k) : "s"(lds), "s"(sb), "v"(voff) : "memory");
}
#define SB() __builtin_amdgcn_sched_barrier(0)
#define PIN2(a, b) asm volatile("" : "+v"(a), "+v"(b))
#define PIN1(a) asm volatile("" : "+v"(a))
DI unsigned lds_base(char* smem) { return (unsigned)(size_t)(LAS char*)smem; }
DI u32x4 lds_r128(const char* p) { return *(const u32x4*)p; }
DI u32x4 lds_tr2(const char* p0, const char* p1) {
  s16x4 a = __builtin_amdgcn_ds_read_tr16_b64_v4i16((LAS s16x4*)p0);
  s16x4 b = __builtin_amdgcn_ds_read_tr16_b64_v4i16((LAS s16x4*)p1);
  u32x2 ua = __builtin_bit_cast(u32x2, a), ub = __builtin_bit_cast(u32x2, b);
  u32x4 r = {ua[0], ua[1], ub[0], ub[1]};
  return r;
}
DI f32x16 mfma_u(u32x4 a, u32x4 b, f32x16 c) {
  return __builtin_amdgcn_mfma_f32_32x32x16_bf16(__builtin_bit_cast(bf16x8, a), __builtin_bit_cast(bf16x8, b), c, 0, 0, 0);
}
DI f32x2 halves(float v) {
  auto r = __builtin_amdgcn_permlane32_swap(__float_as_uint(v), __float_as_uint(v), false, false);
  return (f32x2){__uint_as_float(r[0]), __uint_as_float(r[1])};
}
DI float sum32(float v) { f32x2 t = halves(v); return t[0] + t[1]; }
DI unsigned wave_max_u(unsigned u) {
  u = max(u, (unsigned)__builtin_amdgcn_update_dpp(0, (int)u, 0x128, 0xf, 0xf, false));
  u = max(u, (unsigned)__builtin_amdgcn_update_dpp(0, (int)u, 0x124, 0xf, 0xf, false));
  u = max(u, (unsigned)__builtin_amdgcn_update_dpp(0, (int)u, 0x122, 0xf, 0xf, false));
  u = max(u, (unsigned)__builtin_amdgcn_update_dpp(0, (int)u, 0x121, 0xf, 0xf, false));
  unsigned a = __builtin_amdgcn_readlane(u, 0), b = __builtin_amdgcn_readlane(u, 16);
  unsigned c = __builtin_amdgcn_readlane(u, 32), d = __builtin_amdgcn_readlane(u, 48);
  return max(max(a, b), max(c, d));
}
DI f32x2 ln_scalars(float sum, float sq) {
  const float mean = sum * (1.0f / D_);
  float var = sq * (1.0f / D_) - mean * mean;
  var = var > 0.f ? var : 0.f;
  const float sd = rbf(sqrtf(rbf(rbf(var) + LN_EPS)));
  return (f32x2){rbf(mean), 1.0f / sd};
}

template <int ND, int E4, int E7>
constexpr int ring_allow(int k, int last) {
  int n = (last - k) * ND;
  if (k <= 4 && last >= 4) n += E4;
  if (k <= 7 && last >= 7) n += E7;
  return n;
}
struct NoHook { DI void operator()(int) const {} };
template <int NS, int NBUF, int STG, int ND, int E4 = 0, int E7 = 0, class F, class H = NoHook>
DI void ring_loader(F issue, H idle = H()) {
  issue(0, 0);
  issue(1, STG);
  WAITV(ND);
  BAR();
#pragma unroll
  for (int s = 2; s < NBUF; ++s) issue(s, s * STG);
#pragma unroll
  for (int s = 0; s < NS - 1; ++s) {
    WAITV((ring_allow<ND, E4, E7>(s + 1, s + NBUF - 1 < NS - 1 ? s + NBUF - 1 : NS - 1)));
    BAR();
    if (s + NBUF < NS) issue(s + NBUF, (s % NBUF) * STG);
    idle(s);
  }
}

constexpr int A_XB = 128 * 128, A_WB = 64 * 192, A_STG = A_XB + A_WB, A_NBUF = 4;
constexpr int A_MISC = A_STG * A_NBUF;
constexpr int A_LDS = A_MISC + 5 * 96 * 4 + 128 * 8 + 1024;

template <int GRP, bool ISV>
DI void qkv_body(char* __restrict__ ws, char* smem, int mt, int nt, int wq) {
  TSDECL
  constexpr int NB = GRP == 0 ? 2 : 1, B0 = GRP == 0 ? 0 : 2;
  const int lane = threadIdx.x & 63, r = lane & 31, h = lane >> 5;
  const int off = (84 * nt) & 7;
  f32x16 acc[NB];
  float cs[NB];
#pragma unroll
  for (int b = 0; b < NB; ++b) {
    cs[b] = 0.f;
#pragma unroll
    for (int i = 0; i < 16; ++i) acc[b][i] = 0.f;
  }
  float sum = 0.f, sq = 0.f, sum1 = 0.f, sq1 = 0.f;
  const int xrow = 32 * wq + r, xsw = (xrow >> 1) & 7;
  const int q4 = (lane & 15) >> 2, p4 = lane & 3, blk = (lane >> 4) & 1;
  int ao[4], bo[4];
#pragma unroll
  for (int p = 0; p < 4; ++p) {
    const int kk = (p + wq) & 3;
    ao[p] = xrow * 128 + (((2 * kk + h) ^ xsw) << 4);
    bo[p] = A_XB + (16 * kk + 8 * h + q4) * 192 + (off + 16 * blk + 4 * p4) * 2 + B0 * 56;
  }
  struct Fr { u32x4 a, w[NB]; } f0, f1;
  auto rd = [&](Fr& f, int so, int p) {
    f.a = lds_r128(smem + so + ao[p]);
#pragma unroll
    for (int b = 0; b < NB; ++b) f.w[b] = lds_tr2(smem + so + bo[p] + b * 56, smem + so + bo[p] + b * 56 + 4 * 192);
    SB();
  };
  auto mm = [&](const Fr& f, int p) {
#pragma unroll
    for (int b = 0; b < NB; ++b) acc[b] = ISV ? mfma_u(f.a, f.w[b], acc[b]) : mfma_u(f.w[b], f.a, acc[b]);
    if (GRP == 1) {
#pragma unroll
      for (int i = 0; i < 4; i += 2) {
        sum = dot2(f.a[i], ONES2, sum); sq = dot2(f.a[i], f.a[i], sq);
        sum1 = dot2(f.a[i + 1], ONES2, sum1); sq1 = dot2(f.a[i + 1], f.a[i + 1], sq1);
      }
      PIN2(sum, sq); PIN2(sum1, sq1);
    }
    if (p == 0) {
#pragma unroll
      for (int i = 0; i < 4; ++i)
#pragma unroll
        for (int b = 0; b < NB; ++b) cs[b] = dot2(f.w[b][i], ONES2, cs[b]);
#pragma unroll
      for (int b = 0; b < NB; ++b) PIN1(cs[b]);
    }
    SB();
  };
  BAR();
  TS(2)
  rd(f0, 0, 0);
  int so = 0;
#pragma unroll 1
  for (int s = 0; s < 7; ++s) {
    rd(f1, so, 1); mm(f0, 0);
    rd(f0, so, 2); mm(f1, 1);
    rd(f1, so, 3); mm(f0, 2);
    if (s < 6) {
      WAITL();
      BAR();
      so = so == (A_NBUF - 1) * A_STG ? 0 : so + A_STG;
      rd(f0, so, 0);
    }
    mm(f1, 3);
  }
  TS(9)
  float* csp = (float*)(smem + A_MISC);
  f32x2* tst = (f32x2*)(smem + A_MISC + 5 * 96 * 4);
#pragma unroll
  for (int b = 0; b < NB; ++b) {
    const float c = sum32(cs[b]);
    if (h == 0) csp[wq * 96 + (B0 + b) * 32 + r] = c;
  }
  if (GRP == 1) {
    const f32x2 st = ln_scalars(sum32(sum + sum1), sum32(sq + sq1));
    if (h == 0) tst[32 * wq + r] = st;
  }
  WAITL();
  BAR();
  BAR();
  const float* ct = csp + 384;
  const int tb = mt * 4 + wq;
#pragma unroll
  for (int b = 0; b < NB; ++b) {
    const int gb = 3 * nt + B0 + b, head = gb & 15, type = gb >> 4, cb = (B0 + b) * 32;
    unsigned o[8];
    if (ISV) {
      const float c = ct[cb + r];
#pragma unroll
      for (int g = 0; g < 4; ++g) {
        const float* sp = (const float*)(tst + 32 * wq + 8 * g + 4 * h);
        f32x4 s0 = *(const f32x4*)sp, s1 = *(const f32x4*)(sp + 4);
        float v0 = s0[1] * (acc[b][4 * g] - s0[0] * c), v1 = s0[3] * (acc[b][4 * g + 1] - s0[2] * c);
        float v2 = s1[1] * (acc[b][4 * g + 2] - s1[0] * c), v3 = s1[3] * (acc[b][4 * g + 3] - s1[2] * c);
        o[2 * g] = pk2(v0, v1); o[2 * g + 1] = pk2(v2, v3);
      }
      if (r >= DH_) {
        const unsigned f = (r == DH_) ? ONES2 : 0u;
#pragma unroll
        for (int i = 0; i < 8; ++i) o[i] = f;
      }
      char* dst = ws + WS_KV + (head * 64 + tb) * 4096 + 2048 + lane * 16;
      *(u32x4*)dst = (u32x4){o[0], o[1], o[2], o[3]};
      *(u32x4*)(dst + 1024) = (u32x4){o[4], o[5], o[6], o[7]};
    } else {
      const f32x2 st = tst[32 * wq + r];
      const float sc = type == 0 ? st[1] * QSCALE : st[1];
      float n2 = 0.f;
#pragma unroll
      for (int g = 0; g < 4; ++g) {
        const f32x4 c4 = *(const f32x4*)(ct + cb + 8 * g + 4 * h);
        float v0 = sc * (acc[b][4 * g] - st[0] * c4[0]), v1 = sc * (acc[b][4 * g + 1] - st[0] * c4[1]);
        float v2 = sc * (acc[b][4 * g + 2] - st[0] * c4[2]), v3 = sc * (acc[b][4 * g + 3] - st[0] * c4[3]);
        unsigned a = pk2(v0, v1), bb = pk2(v2, v3);
        if (g == 3 && h == 1) { a = 0u; bb = 0u; }
        o[2 * g] = a; o[2 * g + 1] = bb;
        n2 = dot2(a, a, n2); n2 = dot2(bb, bb, n2);
      }
      char* dst = ws + (type == 0 ? WS_Q + (head * 64 + tb) * 2048 : WS_KV + (head * 64 + tb) * 4096) + lane * 16;
      *(u32x4*)dst = (u32x4){o[0], o[1], o[2], o[3]};
      *(u32x4*)(dst + 1024) = (u32x4){o[4], o[5], o[6], o[7]};
      const unsigned mx = wave_max_u(__float_as_uint(sum32(n2)));
      if (lane == 0) ((unsigned*)(ws + WS_N))[type * 1024 + head * 64 + tb] = mx;
    }
  }
  TSEND(0)
}

__global__ __launch_bounds__(768) void k_qkv(const u16* __restrict__ x, const u16* __restrict__ w, char* __restrict__ ws) {
  extern __shared__ __attribute__((aligned(16))) char smem[];
  const int bid = blockIdx.x, xcd = bid & 7, idx = bid >> 3;
  const int mt = (xcd >> 1) * 4 + (idx >> 3), nt = (xcd & 1) * 8 + (idx & 7);
  const int wv = __builtin_amdgcn_readfirstlane(threadIdx.x >> 6), wq = wv & 3;
  if (wv >= 8) {
    const int lane = threadIdx.x & 63, t = wq * 64 + lane;
    const int ca = (84 * nt) & ~7;
    int sw[3], sx[4];
#pragma unroll
    for (int i = 0; i < 3; ++i) {
      int ci = 64 * (3 * wq + i) + lane, row = ci / 12, c = ci - row * 12;
      int col = ca + 8 * c;
      col = col > (N3_ - 8) ? (N3_ - 8) : col;
      sw[i] = (row * N3_ + col) * 2 - 1024 * i;
    }
#pragma unroll
    for (int i = 0; i < 4; ++i) {
      int c = (lane & 7) ^ (((i & 1) * 4 + (lane >> 4)) & 7);
      sx[i] = ((mt * 128 + 32 * wq + 8 * i + (lane >> 3)) * D_ + 8 * c) * 2 - 1024 * i;
    }
    ring_loader<7, A_NBUF, A_STG, 7>([&](int s, int so) {
      dma3(w + 64 * s * N3_, lds_base(smem) + so + A_XB + 3 * wq * 1024, sw[0], sw[1], sw[2]);
      dma4(x + 64 * s, lds_base(smem) + so + wq * 4096, sx[0], sx[1], sx[2], sx[3]);
    });
    BAR();
    float* csp = (float*)(smem + A_MISC);
    if (t < 96) csp[384 + t] = csp[t] + csp[96 + t] + csp[192 + t] + csp[288 + t];
    WAITL();
    BAR();
  } else if (wv < 4) {
    if (nt >= 11) qkv_body<0, true>(ws, smem, mt, nt, wq);
    else qkv_body<0, false>(ws, smem, mt, nt, wq);
  } else {
    if (nt >= 10) qkv_body<1, true>(ws, smem, mt, nt, wq);
    else qkv_body<1, false>(ws, smem, mt, nt, wq);
  }
}

constexpr int B_LDS = 4 * 32768 + 1024;

template <bool SAFE>
DI void attn_loop(char* smem, const u32x4 (&qf)[2], f32x16& o, float& m, int kh) {
  const int lane = threadIdx.x & 63;
  const f32x16 zero = {};
  BAR();
  const char* lp = smem + kh * 16384 + lane * 16;
  u32x4 kk[2][2], vv[4][2];
  f32x16 sc[2];
  kk[0][0] = lds_r128(lp); kk[0][1] = lds_r128(lp + 1024);
  kk[1][0] = lds_r128(lp + 4096); kk[1][1] = lds_r128(lp + 5120);
  vv[0][0] = lds_r128(lp + 2048); vv[0][1] = lds_r128(lp + 3072);
  vv[1][0] = lds_r128(lp + 6144); vv[1][1] = lds_r128(lp + 7168);
  sc[0] = mfma_u(kk[0][1], qf[1], mfma_u(kk[0][0], qf[0], zero));
  SB();
#pragma unroll 1
  for (int s = 0; s < 8; ++s) {
#pragma unroll
    for (int b = 0; b < 4; ++b) {
      f32x16& cur = sc[b & 1];
      f32x16& nxt = sc[(b + 1) & 1];
      nxt = mfma_u(kk[(b + 1) & 1][0], qf[0], zero);
      SB();
      if (b == 2 && s < 7) {
        WAITL();
        BAR();
      }
      const char* np = lp + (b >= 2 ? ((s + 1) & 3) * 32768 + (b - 2) * 4096 : (s & 3) * 32768 + (b + 2) * 4096);
      kk[b & 1][0] = lds_r128(np); kk[b & 1][1] = lds_r128(np + 1024);
      vv[(b + 2) & 3][0] = lds_r128(np + 2048); vv[(b + 2) & 3][1] = lds_r128(np + 3072);
      SB();
      if (!SAFE) {
        float tm = cur[0];
#pragma unroll
        for (int i = 1; i < 16; ++i) tm = fmaxf(tm, cur[i]);
        const f32x2 t2 = halves(tm);
        const float mn = fmaxf(m, fmaxf(t2[0], t2[1]));
        const float al = __builtin_amdgcn_exp2f(m - mn);
        m = mn;
#pragma unroll
        for (int i = 0; i < 16; ++i) { o[i] *= al; cur[i] -= mn; }
      }
      u32x4 p0, p1;
#pragma unroll
      for (int i = 0; i < 4; ++i) p0[i] = pk2(__builtin_amdgcn_exp2f(cur[2 * i]), __builtin_amdgcn_exp2f(cur[2 * i + 1]));
      SB();
      nxt = mfma_u(kk[(b + 1) & 1][1], qf[1], nxt);
      o = mfma_u(vv[b][0], p0, o);
      SB();
#pragma unroll
      for (int i = 0; i < 4; ++i)
        p1[i] = pk2(__builtin_amdgcn_exp2f(cur[8 + 2 * i]), __builtin_amdgcn_exp2f(cur[8 + 2 * i + 1]));
      SB();
      o = mfma_u(vv[b][1], p1, o);
      SB();
    }
  }
}

__global__ __launch_bounds__(768) void k_attn(char* __restrict__ ws, const u16* __restrict__ wo, const u16* __restrict__ wg,
                                              const u16* __restrict__ wu, const u16* __restrict__ wd) {
  extern __shared__ __attribute__((aligned(16))) char smem[];
  TSDECL
  const int lane = threadIdx.x & 63, wv = __builtin_amdgcn_readfirstlane(threadIdx.x >> 6);
  const int bid = blockIdx.x, xcd = bid & 7, idx = bid >> 3;
  const int head = xcd * 2 + (idx >> 4), qt = idx & 15, qb = wv & 3, kh = wv >> 2, tb = qt * 4 + qb;
  if (wv >= 8) {
    const char* kv = ws + WS_KV + head * (64 * 4096);
    const int t = qb * 64 + lane, l16 = lane * 16;
    const unsigned junk = lds_base(smem) + 4 * 32768 + qb * 256;
    const int t1 = t < 98 ? t : 97, t2 = t < 196 ? t : 195;
    const int go = (idx * 14 + t2 / 14) * (F_ * 2) + (xcd & 1) * 1792 + (t2 % 14) * 128;
    u32x4 cw[4];
    ring_loader<8, 4, 32768, 8, 1, 2>([&](int s, int so) {
      const unsigned dst = lds_base(smem) + so + qb * 8192;
      const char* src = kv + ((qb >> 1) * 32 + s * 4) * 4096 + (qb & 1) * 8192;
      dma4(src, dst, l16, l16, l16, l16);
      dma4(src + 4096, dst + 4096, l16, l16, l16, l16);
      if (s == 4) touch(wo, (idx * 98 + t1) * 128, junk);
      if (s == 7) { touch(wg, go, junk); touch(wu, go, junk); }
    }, [&](int s) {
      if (bid >= 196) return;
      const int rc = (bid / 28) * 4 + qb, cc = bid % 28;
      if (s == 4) {
        const u16* p = (cc < 14 ? wg : wu) + (rc * 16 + (lane >> 4)) * F_ + (cc < 14 ? cc : cc - 14) * 128 + (lane & 15) * 8;
#pragma unroll
        for (int i = 0; i < 4; ++i) cw[i] = *(const u32x4*)(p + 4 * i * F_);
      }
      if (s == 5) {
        float c[8];
#pragma unroll
        for (int i = 0; i < 4; ++i) {
          c[2 * i] = (bf_lo(cw[0][i]) + bf_lo(cw[1][i])) + (bf_lo(cw[2][i]) + bf_lo(cw[3][i]));
          c[2 * i + 1] = (bf_hi(cw[0][i]) + bf_hi(cw[1][i])) + (bf_hi(cw[2][i]) + bf_hi(cw[3][i]));
        }
#pragma unroll
        for (int i = 0; i < 8; ++i) { c[i] = sum32(c[i]); c[i] += __shfl_xor(c[i], 16); }
        f32x4* d = (f32x4*)(ws + WS_CS) + (rc * 28 + cc) * 32 + (lane & 15) * 2;
        if (lane < 16) { d[0] = (f32x4){c[0], c[1], c[2], c[3]}; d[1] = (f32x4){c[4], c[5], c[6], c[7]}; }
      }
    });
    WAITV(0);
    return;
  }
  const char* qp = ws + WS_Q + (head * 64 + tb) * 2048 + lane * 16;
  const u32x4 qf[2] = {*(const u32x4*)qp, *(const u32x4*)(qp + 1024)};
  const unsigned* nn = (const unsigned*)(ws + WS_N) + head * 64 + lane;
  const unsigned nq = nn[0], k2 = wave_max_u(nn[1024]), q2 = wave_max_u((lane >> 2) == qt ? nq : 0u);
  const bool safe = __uint_as_float(k2) * __uint_as_float(q2) <= SAFE_T2;
  f32x16 o = {};
  float m = -3.0e38f;
  TS(2)
  if (safe) attn_loop<true>(smem, qf, o, m, kh);
  else attn_loop<false>(smem, qf, o, m, kh);
  TS(3)
  f32x4* red = (f32x4*)(smem + 32768) + qb * 320 + lane;
  if (kh == 1) {
#pragma unroll
    for (int g = 0; g < 4; ++g) red[g * 64] = (f32x4){o[4 * g], o[4 * g + 1], o[4 * g + 2], o[4 * g + 3]};
    ((float*)(red - lane + 256))[lane] = m;
  }
  WAITL();
  BAR();
  if (kh == 0) {
    float a0 = 1.f, a1 = 1.f;
    if (!safe) {
      const float m1 = ((const float*)(red - lane + 256))[lane], mm = fmaxf(m, m1);
      a0 = __builtin_amdgcn_exp2f(m - mm); a1 = __builtin_amdgcn_exp2f(m1 - mm);
    }
#pragma unroll
    for (int g = 0; g < 4; ++g) {
      const f32x4 t = red[g * 64];
#pragma unroll
      for (int i = 0; i < 4; ++i) o[4 * g + i] = o[4 * g + i] * a0 + t[i] * a1;
    }
    const float inv = 1.0f / halves(o[12])[1];
    unsigned u[8];
#pragma unroll
    for (int i = 0; i < 8; ++i) u[i] = pk2(o[2 * i] * inv, o[2 * i + 1] * inv);
    if (lane >= 32) { u[6] = 0u; u[7] = 0u; }
    char* dst = ws + WS_O + (tb * 16 + head) * 2048 + lane * 16;
    *(u32x4*)dst = (u32x4){u[0], u[1], u[2], u[3]};
    *(u32x4*)(dst + 1024) = (u32x4){u[4], u[5], u[6], u[7]};
  }
  TSEND(1)
}

constexpr int C_LDS = 4 * 32768 + 1024;
template <int MODE>
__global__ __launch_bounds__(768) void k_gemm_res(char* __restrict__ ws, const u16* __restrict__ B, const u16* __restrict__ x,
                                                  u16* __restrict__ out) {
  extern __shared__ __attribute__((aligned(16))) char smem[];
  TSDECL
  constexpr int NS = MODE ? 14 : 4, NBUF = 4, STG = 32768, KS = MODE ? 112 : 32, RPS = MODE ? 128 : 112;
  const int lane = threadIdx.x & 63, wv = __builtin_amdgcn_readfirstlane(threadIdx.x >> 6);
  const int r = lane & 31, h = lane >> 5;
  const int bid = blockIdx.x, xcd = bid & 7, idx = bid >> 3;
  const int mt = xcd * 4 + idx / 7, nt = idx % 7;
  if (wv >= 8) {
    const int lw = wv - 8, l16 = lane * 16;
    const char* A0 = ws + (MODE ? WS_ACT : WS_O) + (size_t)mt * 2 * KS * 1024;
    const char* A = A0 + (lw >> 1) * KS * 1024 + (lw & 1) * 4096;
    int bs[4];
#pragma unroll
    for (int i = 0; i < 4; ++i) {
      int row = 32 * lw + 8 * i + (lane >> 3);
      const int cb = (lane & 7) ^ (((row >> 1) & 1) << 2);
      if (MODE == 0 && row >= 112) row -= 16;
      bs[i] = (row * D_ + nt * 64 + 8 * cb) * 2 - 1024 * i;
    }
    ring_loader<NS, NBUF, STG, 8>([&](int s, int so) {
      const unsigned buf = lds_base(smem) + so + lw * 4096;
      dma4(B + s * RPS * D_, buf + 16384, bs[0], bs[1], bs[2], bs[3]);
      dma4(A + s * 8192, buf, l16, l16, l16, l16);
    });
    return;
  }
  const int tbl = wv & 1, cbk = (wv >> 1) & 1, kh = wv >> 2;
  u32x4 rv[2];
  char* hp = ws + WS_H + ((mt * 2 + tbl) * 28 + nt * 4 + cbk * 2) * 1024 + lane * 16;
  if (kh == 0) {
    if (MODE) { rv[0] = *(const u32x4*)hp; rv[1] = *(const u32x4*)(hp + 1024); }
    else {
      const u16* rp = x + (size_t)(mt * 64 + tbl * 32 + r) * D_ + nt * 64 + cbk * 32 + 4 * h;
#pragma unroll
      for (int g = 0; g < 4; ++g) { u32x2 t = *(const u32x2*)(rp + 8 * g); rv[g >> 1][2 * (g & 1)] = t[0]; rv[g >> 1][2 * (g & 1) + 1] = t[1]; }
    }
  }
  f32x16 acc = {};
  const int q4 = (lane & 15) >> 2, p4 = lane & 3, blk = (lane >> 4) & 1;
  const int bcol = 32 * cbk + 16 * blk + 4 * p4;
  const int aoff = tbl * 8192 + kh * 4096 + lane * 16;
  const int boff = 16384 + ((MODE ? 64 : 56) * kh + 4 * h + q4) * 128 + (((bcol >> 3) ^ ((q4 >> 1) << 2)) << 4) + (bcol & 7) * 2;
  struct Fr { u32x4 a[4], w[4]; } f0, f1;
  auto rd = [&](Fr& f, int so) {
#pragma unroll
    for (int ks = 0; ks < 4; ++ks) {
      f.a[ks] = lds_r128(smem + so + aoff + ks * 1024);
      const char* p = smem + so + boff + (MODE ? 16 * ks : 28 * (ks >> 1) + 16 * (ks & 1)) * 128;
      f.w[ks] = lds_tr2(p, p + 8 * 128);
    }
    SB();
  };
  auto mm = [&](const Fr& f) {
#pragma unroll
    for (int ks = 0; ks < 4; ++ks) acc = mfma_u(f.w[ks], f.a[ks], acc);
    SB();
  };
  BAR();
  TS(2)
  rd(f0, 0);
#pragma unroll 1
  for (int s = 0; s < NS; s += 2) {
    WAITL();
    BAR();
    rd(f1, ((s + 1) & 3) * STG); mm(f0);
    if (s + 2 < NS) { WAITL(); BAR(); rd(f0, ((s + 2) & 3) * STG); }
    mm(f1);
  }
  TS(9)
  char* fr = smem + (NS % NBUF) * STG;
  f32x4* red = (f32x4*)fr + (wv & 3) * 256 + lane;
  if (kh == 1) {
#pragma unroll
    for (int g = 0; g < 4; ++g) red[g * 64] = (f32x4){acc[4 * g], acc[4 * g + 1], acc[4 * g + 2], acc[4 * g + 3]};
  }
  WAITL();
  BAR();
  if (kh == 0) {
    unsigned u[8];
#pragma unroll
    for (int g = 0; g < 4; ++g) {
      const f32x4 t = red[g * 64];
      const unsigned t0 = pk2(acc[4 * g] + t[0], acc[4 * g + 1] + t[1]), t1 = pk2(acc[4 * g + 2] + t[2], acc[4 * g + 3] + t[3]);
      const unsigned r0 = rv[g >> 1][2 * (g & 1)], r1 = rv[g >> 1][2 * (g & 1) + 1];
      u[2 * g] = pk2(bf_lo(t0) + bf_lo(r0), bf_hi(t0) + bf_hi(r0));
      u[2 * g + 1] = pk2(bf_lo(t1) + bf_lo(r1), bf_hi(t1) + bf_hi(r1));
    }
    if (MODE == 0) {
      *(u32x4*)hp = (u32x4){u[0], u[1], u[2], u[3]};
      *(u32x4*)(hp + 1024) = (u32x4){u[4], u[5], u[6], u[7]};
      float s0 = 0.f, s1 = 0.f, q0 = 0.f, q1 = 0.f;
#pragma unroll
      for (int i = 0; i < 8; i += 2) {
        s0 = dot2(u[i], ONES2, s0); q0 = dot2(u[i], u[i], q0);
        s1 = dot2(u[i + 1], ONES2, s1); q1 = dot2(u[i + 1], u[i + 1], q1);
      }
      const f32x2 st = {sum32(s0 + s1), sum32(q0 + q1)};
      if (h == 0) ((f32x2*)(ws + WS_ST))[((mt * 2 + tbl) * 14 + nt * 2 + cbk) * 32 + r] = st;
    } else {
      char* tp = fr + 16384 + (tbl * 32 + r) * 136 + cbk * 64 + h * 8;
#pragma unroll
      for (int g = 0; g < 4; ++g) *(u32x2*)(tp + 16 * g) = (u32x2){u[2 * g], u[2 * g + 1]};
    }
  }
  if (MODE == 1) {
    WAITL();
    BAR();
    const int row = wv * 8 + (lane >> 3);
    const char* tp = fr + 16384 + row * 136 + (lane & 7) * 16;
    const u32x2 a = *(const u32x2*)tp, b = *(const u32x2*)(tp + 8);
    *(u32x4*)(out + (size_t)(mt * 64 + row) * D_ + nt * 64 + (lane & 7) * 8) = (u32x4){a[0], a[1], b[0], b[1]};
  }
  TSEND(2 + 2 * MODE)
}

constexpr int D_U = 16384 + 32, D_STG = 16384 + 2 * D_U, D_NBUF = 3, D_LDS = D_STG * D_NBUF + 1024;
#define SG(m, n) __builtin_amdgcn_sched_group_barrier(m, n, 0)

__global__ __launch_bounds__(512) void k_ffn1(char* __restrict__ ws, const u16* __restrict__ wg, const u16* __restrict__ wu,
                                              const u16* __restrict__ wd) {
  extern __shared__ __attribute__((aligned(16))) char smem[];
  TSDECL
  const int wv = __builtin_amdgcn_readfirstlane(threadIdx.x >> 6), wq = wv & 3;
  const int bid = blockIdx.x, xcd = bid & 7, idx = bid >> 3;
  const int mt = (xcd >> 1) * 4 + idx / 7, nt = (xcd & 1) * 7 + idx % 7;
  const int lane = threadIdx.x & 63, r = lane & 31, h = lane >> 5;
  const int tb = mt * 4 + wq;
  float* cs = (float*)(smem + D_STG * D_NBUF);
  if (wv >= 4) {
    int src[4];
#pragma unroll
    for (int i = 0; i < 4; ++i)
      src[i] = ((wq & 1) * 32 + 4 * i + (lane >> 4)) * (F_ * 2) + nt * 256 + (((lane & 15) ^ ((lane >> 4) << 2)) << 4) - 1024 * i;
    const char* wsrc = (const char*)(wq < 2 ? wg : wu);
    const char* hsrc = ws + WS_H + tb * (28 * 1024);
    const int l16 = lane * 16, t = wq * 64 + lane;
    float cp[28];
    ring_loader<7, D_NBUF, D_STG, 12>([&](int s, int so) {
      const unsigned buf = lds_base(smem) + so + 16384 + (wq >> 1) * D_U + (wq & 1) * 8192;
      dma4(hsrc + s * 4096, lds_base(smem) + so + wq * 4096, l16, l16, l16, l16);
      dma4(wsrc + s * (64 * F_ * 2), buf, src[0], src[1], src[2], src[3]);
      dma4(wsrc + (s * 64 + 16) * (F_ * 2), buf + 4096, src[0], src[1], src[2], src[3]);
    }, [&](int s) {
      if (s == 3) {
        const float* p = (const float*)(ws + WS_CS) + ((t >> 4) & 1) * F_ + nt * 128 + (t >> 5) * 16 + (t & 15);
#pragma unroll
        for (int i = 0; i < 28; ++i) cp[i] = p[i * 2 * F_];
      }
      if (s == 5) {
        const unsigned junk = lds_base(smem) + (5 % D_NBUF) * D_STG + wq * 256;
        touch(wd, (idx * 448 + t) * 128, junk);
        touch(wd, (idx * 448 + 192 + t) * 128, junk);
        WAITV(0);
      }
      if (s == 4) {
        float c = 0.f;
#pragma unroll
        for (int i = 0; i < 28; ++i) c = fma1(cp[i], 1.0f, c);
        cs[t] = c;
      }
    });
    return;
  }
  const f32x2* sp = (const f32x2*)(ws + WS_ST) + (tb * 14 + 7 * h) * 32 + r;
  f32x2 ps[7];
#pragma unroll
  for (int i = 0; i < 7; ++i) ps[i] = sp[i * 32];
  const int q4 = (lane & 15) >> 2, p4 = lane & 3, blk = (lane >> 4) & 1;
  int bo[4];
#pragma unroll
  for (int i = 0; i < 4; ++i) bo[i] = 16384 + blk * D_U + (4 * h + q4) * 256 + ((i ^ q4) << 6) + 8 * p4;
  const char* ap = smem + wq * 4096 + lane * 16;
  f32x16 acc[8];
#pragma unroll
  for (int b = 0; b < 8; ++b)
#pragma unroll
    for (int i = 0; i < 16; ++i) acc[b][i] = 0.f;
  const f32x2 tot = ((ps[0] + ps[1]) + (ps[2] + ps[3])) + ((ps[4] + ps[5]) + ps[6]);
  f32x2 st = ln_scalars(sum32(tot[0]), sum32(tot[1]));
  PIN1(st);
  u32x4 w[2][8], a[2];
  SB();
  BAR();
  TS(2)
  a[0] = lds_r128(ap);
#pragma unroll
  for (int b = 0; b < 8; ++b) w[0][b] = lds_tr2(smem + bo[b >> 1] + (b & 1) * 32, smem + bo[b >> 1] + (b & 1) * 32 + 2048);
  SB();
  int so = 0;
#pragma unroll 1
  for (int s = 0; s < 7; ++s) {
#pragma unroll
    for (int j = 0; j < 4; ++j) {
      const int c = j & 1, n = c ^ 1, jn = (j + 1) & 3;
      __builtin_amdgcn_s_waitcnt(0xc07f);
      if (j == 3) {
        acc[0] = mfma_u(w[c][0], a[c], acc[0]);
        SB();
        if (s < 6) BAR();
        so = so == (D_NBUF - 1) * D_STG ? 0 : so + D_STG;
        SB();
      }
      const char* p = smem + so + jn * 4096;
      a[n] = lds_r128(ap + so + jn * 1024);
#pragma unroll
      for (int b = 0; b < 8; ++b) w[n][b] = lds_tr2(p + bo[b >> 1] + (b & 1) * 32, p + bo[b >> 1] + (b & 1) * 32 + 2048);
#pragma unroll
      for (int b = j == 3 ? 1 : 0; b < 8; ++b) acc[b] = mfma_u(w[c][b], a[c], acc[b]);
      if (j == 3) SG(0x100, 4);
#pragma unroll
      for (int b = 0; b < 5; ++b) { SG(0x008, 1); SG(0x100, 4); }
      SG(0x008, 3);
      SB();
    }
  }
  TS(9)
  const float k1 = st[1] * (-2.0f * 0.7978845608028654f * 1.4426950408889634f);
  const float k3 = k1 * st[1] * st[1] * 0.044715f, i2 = 1.0f / (st[1] * st[1]);
  const f32x2 c1 = {k1, k1}, c3 = {k3, k3}, ir2 = {i2, i2}, nm = {-st[0], -st[0]};
  char* dst = ws + WS_ACT + (size_t)(tb * 112 + nt * 8) * 1024 + lane * 16;
#pragma unroll
  for (int b = 0; b < 8; ++b) {
    u32x4 o;
    f32x4 c4[4];
#pragma unroll
    for (int g = 0; g < 4; ++g) c4[g] = *(const f32x4*)(cs + b * 32 + 8 * g + 4 * h);
#pragma unroll
    for (int i = 0; i < 4; ++i) {
      const int q = i >> 1, e = (i & 1) * 2;
      const f32x2 g = __builtin_elementwise_fma(nm, (f32x2){c4[q][e], c4[q][e + 1]}, (f32x2){acc[b][2 * i], acc[b][2 * i + 1]});
      const f32x2 u = __builtin_elementwise_fma(nm, (f32x2){c4[2 + q][e], c4[2 + q][e + 1]}, (f32x2){acc[b][8 + 2 * i], acc[b][9 + 2 * i]});
      const f32x2 z = __builtin_elementwise_fma(g * g, c3, c1) * g;
      const f32x2 d = __builtin_elementwise_fma((f32x2){__builtin_amdgcn_exp2f(z[0]), __builtin_amdgcn_exp2f(z[1])}, ir2, ir2);
      const f32x2 v = (g * u) * (f32x2){__builtin_amdgcn_rcpf(d[0]), __builtin_amdgcn_rcpf(d[1])};
      o[i] = pk2(v[0], v[1]);
    }
    *(u32x4*)(dst + b * 1024) = o;
  }
  TSEND(3)
}

extern "C" void kernel_launch(void* const* d_in, const int* in_sizes, int n_in, void* d_out, int out_size, void* d_ws,
                              size_t ws_size, hipStream_t stream) {
  const u16* x = (const u16*)d_in[0];
  const u16* wqkv = (const u16*)d_in[1];
  const u16* wo = (const u16*)d_in[2];
  const u16* wg = (const u16*)d_in[4];
  const u16* wu = (const u16*)d_in[5];
  const u16* wd = (const u16*)d_in[6];
  u16* out = (u16*)d_out;
  char* ws = (char*)d_ws;
  (void)hipFuncSetAttribute((const void*)k_qkv, hipFuncAttributeMaxDynamicSharedMemorySize, A_LDS);
  (void)hipFuncSetAttribute((const void*)k_attn, hipFuncAttributeMaxDynamicSharedMemorySize, B_LDS);
  (void)hipFuncSetAttribute((const void*)k_gemm_res<0>, hipFuncAttributeMaxDynamicSharedMemorySize, C_LDS);
  (void)hipFuncSetAttribute((const void*)k_gemm_res<1>, hipFuncAttributeMaxDynamicSharedMemorySize, C_LDS);
  (void)hipFuncSetAttribute((const void*)k_ffn1, hipFuncAttributeMaxDynamicSharedMemorySize, D_LDS);
  k_qkv<<<256, 768, A_LDS, stream>>>(x, wqkv, ws);
  k_attn<<<256, 768, B_LDS, stream>>>(ws, wo, wg, wu, wd);
  k_gemm_res<0><<<224, 768, C_LDS, stream>>>(ws, wo, x, out);
  k_ffn1<<<224, 512, D_LDS, stream>>>(ws, wg, wu, wd);
  k_gemm_res<1><<<224, 768, C_LDS, stream>>>(ws, wd, x, out);
}
```

```cpp
#include <hip/hip_runtime.h>
#include <hip/hip_bf16.h>

typedef unsigned short u16;
typedef __attribute__((ext_vector_type(8))) short bf16x8;
typedef __attribute__((ext_vector_type(4))) short s16x4;
typedef __attribute__((ext_vector_type(2))) __bf16 b16x2;
typedef __attribute__((ext_vector_type(2))) float f32x2;
typedef __attribute__((ext_vector_type(4))) float f32x4;
typedef __attribute__((ext_vector_type(16))) float f32x16;
typedef __attribute__((ext_vector_type(4))) unsigned u32x4;
typedef __attribute__((ext_vector_type(2))) unsigned u32x2;
#define LAS __attribute__((address_space(3)))
#define DI __device__ __forceinline__
#define WAITV(n) asm volatile("s_waitcnt vmcnt(%0) lgkmcnt(0)" ::"i"(n) : "memory")
#define WAITL() asm volatile("s_waitcnt lgkmcnt(0)" ::: "memory")
#define BAR() __builtin_amdgcn_s_barrier()
#define TSDECL
#define TS(i)
#define TSEND(kid)

constexpr int D_ = 448, DH_ = 28, F_ = 1792, N3_ = 1344;
constexpr float LN_EPS = 1e-5f;
constexpr float QSCALE = 0.18898223650461362f * 1.4426950408889634f;
constexpr float SAFE_T2 = 3600.0f;
constexpr unsigned WS_Q = 0, WS_KV = 2u << 20, WS_O = 6u << 20, WS_H = 8u << 20, WS_ACT = 10u << 20, WS_N = 18u << 20, WS_ST = 19u << 20, WS_CS = 20u << 20, WS_CNT = 21u << 20;

DI unsigned pk2(float lo, float hi) {
  f32x2 f = {lo, hi};
  return __builtin_bit_cast(unsigned, __builtin_convertvector(f, b16x2));
}
DI float bf_lo(unsigned u) { return __uint_as_float(u << 16); }
DI float bf_hi(unsigned u) { return __uint_as_float(u & 0xffff0000u); }
DI float rbf(float v) { return bf_lo(pk2(v, v)); }
DI float dot2(unsigned a, unsigned b, float c) {
  return __builtin_amdgcn_fdot2_f32_bf16(__builtin_bit_cast(b16x2, a), __builtin_bit_cast(b16x2, b), c, false);
}
DI float fma1(float a, float b, float c) {
  float d;
  asm("v_fma_f32 %0, %1, %2, %3" : "=v"(d) : "v"(a), "v"(b), "v"(c));
  return d;
}
constexpr unsigned ONES2 = 0x3f803f80u;
#define DMA_HEAD "s_mov_b32 %0, m0\n\ts_mov_b32 m0, %1\n\ts_nop 0\n\t"
#define DMA_L(v, o) "global_load_lds_dwordx4 %" #v ", %2 offset:" #o "\n\t"
#define DMA_TAIL "s_mov_b32 m0, %0"
DI void dma2(const void* sb, unsigned lds, int a, int b) {
  unsigned k;
  asm volatile(DMA_HEAD DMA_L(3, 0) DMA_L(4, 1024) DMA_TAIL : "=&s"(k) : "s"(lds), "s"(sb), "v"(a), "v"(b) : "memory");
}
DI void dma3(const void* sb, unsigned lds, int a, int b, int c) {
  unsigned k;
  asm volatile(DMA_HEAD DMA_L(3, 0) DMA_L(4, 1024) DMA_L(5, 2048) DMA_TAIL
               : "=&s"(k) : "s"(lds), "s"(sb), "v"(a), "v"(b), "v"(c) : "memory");
}
DI void dma4(const void* sb, unsigned lds, int a, int b, int c, int d) {
  unsigned k;
  asm volatile(DMA_HEAD DMA_L(3, 0) DMA_L(4, 1024) DMA_L(5, 2048) DMA_L(6, 3072) DMA_TAIL
               : "=&s"(k) : "s"(lds), "s"(sb), "v"(a), "v"(b), "v"(c), "v"(d) : "memory");
}
DI void touch(const void* sb, int voff, unsigned lds) {
  unsigned k;
  asm volatile(DMA_HEAD "global_load_lds_dword %3, %2\n\t" DMA_TAIL : "=&s"(k) : "s"(lds), "s"(sb), "v"(voff) : "memory");
}
#define SB() __builtin_amdgcn_sched_barrier(0)
#define SG(m, n) __builtin_amdgcn_sched_group_barrier(m, n, 0)
#define PIN2(a, b) asm volatile("" : "+v"(a), "+v"(b))
#define PIN1(a) asm volatile("" : "+v"(a))
DI unsigned lds_base(char* smem) { return (unsigned)(size_t)(LAS char*)smem; }
DI u32x4 lds_r128(const char* p) { return *(const u32x4*)p; }
DI u32x4 lds_tr2(const char* p0, const char* p1) {
  s16x4 a = __builtin_amdgcn_ds_read_tr16_b64_v4i16((LAS s16x4*)p0);
  s16x4 b = __builtin_amdgcn_ds_read_tr16_b64_v4i16((LAS s16x4*)p1);
  u32x2 ua = __builtin_bit_cast(u32x2, a), ub = __builtin_bit_cast(u32x2, b);
  u32x4 r = {ua[0], ua[1], ub[0], ub[1]};
  return r;
}
DI f32x16 mfma_u(u32x4 a, u32x4 b, f32x16 c) {
  return __builtin_amdgcn_mfma_f32_32x32x16_bf16(__builtin_bit_cast(bf16x8, a), __builtin_bit_cast(bf16x8, b), c, 0, 0, 0);
}
DI f32x2 halves(float v) {
  auto r = __builtin_amdgcn_permlane32_swap(__float_as_uint(v), __float_as_uint(v), false, false);
  return (f32x2){__uint_as_float(r[0]), __uint_as_float(r[1])};
}
DI float sum32(float v) { f32x2 t = halves(v); return t[0] + t[1]; }
DI unsigned wave_max_u(unsigned u) {
  u = max(u, (unsigned)__builtin_amdgcn_update_dpp(0, (int)u, 0x128, 0xf, 0xf, false));
  u = max(u, (unsigned)__builtin_amdgcn_update_dpp(0, (int)u, 0x124, 0xf, 0xf, false));
  u = max(u, (unsigned)__builtin_amdgcn_update_dpp(0, (int)u, 0x122, 0xf, 0xf, false));
  u = max(u, (unsigned)__builtin_amdgcn_update_dpp(0, (int)u, 0x121, 0xf, 0xf, false));
  unsigned a = __builtin_amdgcn_readlane(u, 0), b = __builtin_amdgcn_readlane(u, 16);
  unsigned c = __builtin_amdgcn_readlane(u, 32), d = __builtin_amdgcn_readlane(u, 48);
  return max(max(a, b), max(c, d));
}
DI void spin_until(unsigned* c, unsigned n) {
  while (__hip_atomic_load(c, __ATOMIC_RELAXED, __HIP_MEMORY_SCOPE_AGENT) < n) __builtin_amdgcn_s_sleep(2);
}
DI f32x2 ln_scalars(float sum, float sq) {
  const float mean = sum * (1.0f / D_);
  float var = sq * (1.0f / D_) - mean * mean;
  var = var > 0.f ? var : 0.f;
  const float sd = rbf(sqrtf(rbf(rbf(var) + LN_EPS)));
  return (f32x2){rbf(mean), 1.0f / sd};
}

template <int ND, int E4, int E7>
constexpr int ring_allow(int k, int last) {
  int n = (last - k) * ND;
  if (k <= 4 && last >= 4) n += E4;
  if (k <= 7 && last >= 7) n += E7;
  return n;
}
#define RP 2
#define RR 2
template <int NS, int NBUF>
constexpr int ring_cnt(int s) {
  int n = RP + (NBUF == 5 ? 1 : RR) * (s + 1);
  n = n < s + NBUF + (s < 0) ? n : s + NBUF + (s < 0);
  return n < NS ? n : NS;
}
struct NoHook { DI void operator()(int) const {} };
template <int NS, int NBUF, int STG, int ND, int E4 = 0, int E7 = 0, class F, class H = NoHook>
DI void ring_loader(F issue, H idle = H()) {
#pragma unroll
  for (int s = -1; s < NS - 1; ++s) {
#pragma unroll
    for (int k = s < 0 ? 0 : ring_cnt<NS, NBUF>(s - 1); k < ring_cnt<NS, NBUF>(s); ++k) issue(k, (k % NBUF) * STG);
    if (s > 0) idle(s - 1);
    WAITV((ring_allow<ND, E4, E7>(s + 1, ring_cnt<NS, NBUF>(s) - 1)));
    BAR();
  }
  idle(NS - 2);
}

constexpr int A_XB = 128 * 128, A_WB = 64 * 192, A_STG = A_XB + A_WB, A_NBUF = 5;
constexpr int A_MISC = A_STG * A_NBUF;
constexpr int A_LDS = A_MISC + 5 * 96 * 4 + 3 * 128 * 8 + 256;
#define TSLEEP 20

template <int GRP, bool ISV>
DI void qkv_body(char* __restrict__ ws, char* smem, int mt, int nt, int wq) {
  TSDECL
  constexpr int NB = GRP == 0 ? 2 : 1, B0 = GRP == 0 ? 0 : 2;
  const int lane = threadIdx.x & 63, r = lane & 31, h = lane >> 5;
  const int off = (84 * nt) & 7;
  f32x16 acc[NB];
  float cs[NB];
#pragma unroll
  for (int b = 0; b < NB; ++b) {
    cs[b] = 0.f;
#pragma unroll
    for (int i = 0; i < 16; ++i) acc[b][i] = 0.f;
  }
  float sum = 0.f, sq = 0.f, sum1 = 0.f, sq1 = 0.f;
  const int xrow = 32 * wq + r, xsw = (xrow >> 1) & 7;
  const int q4 = (lane & 15) >> 2, p4 = lane & 3, blk = (lane >> 4) & 1;
  int ao[4], bo[4];
#pragma unroll
  for (int p = 0; p < 4; ++p) {
    const int kk = (p + wq) & 3;
    ao[p] = xrow * 128 + (((2 * kk + h) ^ xsw) << 4);
    bo[p] = A_XB + (16 * kk + 8 * h + q4) * 192 + (off + 16 * blk + 4 * p4) * 2 + B0 * 56;
  }
  struct Fr { u32x4 a[4], w[4][NB]; } f0, f1;
  auto rd = [&](Fr& f, int so) {
#pragma unroll
    for (int p = 0; p < 4; ++p) {
      f.a[p] = lds_r128(smem + so + ao[p]);
#pragma unroll
      for (int b = 0; b < NB; ++b) f.w[p][b] = lds_tr2(smem + so + bo[p] + b * 56, smem + so + bo[p] + b * 56 + 4 * 192);
    }
  };
  auto mm = [&](const Fr& f) {
#pragma unroll
    for (int p = 0; p < 4; ++p)
#pragma unroll
      for (int b = 0; b < NB; ++b) acc[b] = ISV ? mfma_u(f.a[p], f.w[p][b], acc[b]) : mfma_u(f.w[p][b], f.a[p], acc[b]);
#pragma unroll
    for (int i = 0; i < 4 * NB; ++i) { SG(0x008, 1); SG(0x100, 3); }
    SB();
#pragma unroll
    for (int p = GRP; p < (GRP ? 4 : 1); ++p) {
#pragma unroll
      for (int i = 0; i < 4; i += 2) {
        sum = dot2(f.a[p][i], ONES2, sum); sq = dot2(f.a[p][i], f.a[p][i], sq);
        sum1 = dot2(f.a[p][i + 1], ONES2, sum1); sq1 = dot2(f.a[p][i + 1], f.a[p][i + 1], sq1);
      }
    }
    PIN2(sum, sq); PIN2(sum1, sq1);
#pragma unroll
    for (int i = 0; i < 4; ++i)
#pragma unroll
      for (int b = 0; b < NB; ++b) cs[b] = dot2(f.w[0][b][i], ONES2, cs[b]);
#pragma unroll
    for (int b = 0; b < NB; ++b) PIN1(cs[b]);
    SB();
  };
  BAR();
  TS(2)
  rd(f0, 0);
  SB();
  int so = 0;
#pragma unroll 1
  for (int s = 0; s < 6; s += 2) {
    __builtin_amdgcn_s_waitcnt(0xc07f);
    BAR();
    SB();
    so = so == (A_NBUF - 1) * A_STG ? 0 : so + A_STG;
    rd(f1, so); mm(f0);
    __builtin_amdgcn_s_waitcnt(0xc07f);
    BAR();
    SB();
    so = so == (A_NBUF - 1) * A_STG ? 0 : so + A_STG;
    rd(f0, so); mm(f1);
  }
  mm(f0);
  TS(9)
  float* csp = (float*)(smem + A_MISC);
  f32x2* tst = (f32x2*)(smem + A_MISC + 5 * 96 * 4);
#pragma unroll
  for (int b = 0; b < NB; ++b) {
    const float c = sum32(cs[b]);
    if (h == 0) csp[wq * 96 + (B0 + b) * 32 + r] = c;
  }
  {
    const f32x2 pr = {sum32(sum + sum1), sum32(sq + sq1)};
    if (h == 0) tst[128 + GRP * 128 + 32 * wq + r] = pr;
  }
  WAITL();
  BAR();
  BAR();
  const float* ct = csp + 384;
  const int tb = mt * 4 + wq;
#pragma unroll
  for (int b = 0; b < NB; ++b) {
    const int gb = 3 * nt + B0 + b, head = gb & 15, type = gb >> 4, cb = (B0 + b) * 32;
    unsigned o[8];
    if (ISV) {
      const float c = ct[cb + r];
#pragma unroll
      for (int g = 0; g < 4; ++g) {
        const float* sp = (const float*)(tst + 32 * wq + 8 * g + 4 * h);
        f32x4 s0 = *(const f32x4*)sp, s1 = *(const f32x4*)(sp + 4);
        float v0 = s0[1] * (acc[b][4 * g] - s0[0] * c), v1 = s0[3] * (acc[b][4 * g + 1] - s0[2] * c);
        float v2 = s1[1] * (acc[b][4 * g + 2] - s1[0] * c), v3 = s1[3] * (acc[b][4 * g + 3] - s1[2] * c);
        o[2 * g] = pk2(v0, v1); o[2 * g + 1] = pk2(v2, v3);
      }
      if (r >= DH_) {
        const unsigned f = (r == DH_) ? ONES2 : 0u;
#pragma unroll
        for (int i = 0; i < 8; ++i) o[i] = f;
      }
      char* dst = ws + WS_KV + (head * 64 + tb) * 4096 + 2048 + lane * 16;
      *(u32x4*)dst = (u32x4){o[0], o[1], o[2], o[3]};
      *(u32x4*)(dst + 1024) = (u32x4){o[4], o[5], o[6], o[7]};
    } else {
      const f32x2 st = tst[32 * wq + r];
      const float sc = type == 0 ? st[1] * QSCALE : st[1];
      float n2 = 0.f;
#pragma unroll
      for (int g = 0; g < 4; ++g) {
        const f32x4 c4 = *(const f32x4*)(ct + cb + 8 * g + 4 * h);
        float v0 = sc * (acc[b][4 * g] - st[0] * c4[0]), v1 = sc * (acc[b][4 * g + 1] - st[0] * c4[1]);
        float v2 = sc * (acc[b][4 * g + 2] - st[0] * c4[2]), v3 = sc * (acc[b][4 * g + 3] - st[0] * c4[3]);
        unsigned a = pk2(v0, v1), bb = pk2(v2, v3);
        if (g == 3 && h == 1) { a = 0u; bb = 0u; }
        o[2 * g] = a; o[2 * g + 1] = bb;
        n2 = dot2(a, a, n2); n2 = dot2(bb, bb, n2);
      }
      char* dst = ws + (type == 0 ? WS_Q + (head * 64 + tb) * 2048 : WS_KV + (head * 64 + tb) * 4096) + lane * 16;
      *(u32x4*)dst = (u32x4){o[0], o[1], o[2], o[3]};
      *(u32x4*)(dst + 1024) = (u32x4){o[4], o[5], o[6], o[7]};
      const unsigned mx = wave_max_u(__float_as_uint(sum32(n2)));
      if (lane == 0) ((unsigned*)(ws + WS_N))[type * 1024 + head * 64 + tb] = mx;
    }
  }
  TSEND(0)
}

__global__ __launch_bounds__(768) void k_qkv(const u16* __restrict__ x, const u16* __restrict__ w, char* __restrict__ ws) {
  extern __shared__ __attribute__((aligned(16))) char smem[];
  const int bid = blockIdx.x, xcd = bid & 7, idx = bid >> 3;
  const int mt = (xcd >> 1) * 4 + (idx >> 3), nt = (xcd & 1) * 8 + (idx & 7);
  const int wv = __builtin_amdgcn_readfirstlane(threadIdx.x >> 6), wq = wv & 3;
  if (wv >= 8) {
    const int lane = threadIdx.x & 63, t = wq * 64 + lane;
    if (bid == 0 && t < 32) ((unsigned*)(ws + WS_CNT))[t * 16] = 0u;
    const int ca = (84 * nt) & ~7;
    int sw[3], sx[4];
#pragma unroll
    for (int i = 0; i < 3; ++i) {
      int ci = 64 * (3 * wq + i) + lane, row = ci / 12, c = ci - row * 12;
      int col = ca + 8 * c;
      col = col > (N3_ - 8) ? (N3_ - 8) : col;
      sw[i] = (row * N3_ + col) * 2 - 1024 * i;
    }
#pragma unroll
    for (int i = 0; i < 4; ++i) {
      int c = (lane & 7) ^ (((i & 1) * 4 + (lane >> 4)) & 7);
      sx[i] = ((mt * 128 + 32 * wq + 8 * i + (lane >> 3)) * D_ + 8 * c) * 2 - 1024 * i;
    }
    ring_loader<7, A_NBUF, A_STG, 7>([&](int s, int so) {
      dma3(w + 64 * s * N3_, lds_base(smem) + so + A_XB + 3 * wq * 1024, sw[0], sw[1], sw[2]);
      dma4(x + 64 * s, lds_base(smem) + so + wq * 4096, sx[0], sx[1], sx[2], sx[3]);
    });
    BAR();
    float* csp = (float*)(smem + A_MISC);
    f32x2* tst = (f32x2*)(smem + A_MISC + 5 * 96 * 4);
    if (t < 96) csp[384 + t] = csp[t] + csp[96 + t] + csp[192 + t] + csp[288 + t];
    if (t >= 128) {
      const f32x2 p = tst[t] + tst[t + 128];
      tst[t - 128] = ln_scalars(p[0], p[1]);
    }
    WAITL();
    BAR();
    return;
  }
  if (wv < 5) {
    __builtin_amdgcn_s_sleep(TSLEEP);
    const int t = threadIdx.x;
    const unsigned junk = lds_base(smem) + A_LDS - 256;
    const int t1 = t < 112 ? t : 111, t2 = t - 128 < 154 ? t - 128 : 153;
    if (wv < 2) touch(x, ((xcd >> 1) * 512 + idx * 16) * (D_ * 2) + t1 * 128, junk);
    else touch(w, (idx * 14 + t2 / 11) * (N3_ * 2) + ((xcd & 1) * 10 + t2 % 11) * 128, junk);
  }
  if (wv < 4) {
    if (nt >= 11) qkv_body<0, true>(ws, smem, mt, nt, wq);
    else qkv_body<0, false>(ws, smem, mt, nt, wq);
  } else {
    if (nt >= 10) qkv_body<1, true>(ws, smem, mt, nt, wq);
    else qkv_body<1, false>(ws, smem, mt, nt, wq);
  }
}

template <int MODE>
DI void gemm_bsrc(int (&bs)[4], int lw, int lane, int nt) {
#pragma unroll
  for (int i = 0; i < 4; ++i) {
    int row = 32 * lw + 8 * i + (lane >> 3);
    const int cb = (lane & 7) ^ (((row >> 1) & 1) << 2);
    if (MODE == 0 && row >= 112) row -= 16;
    bs[i] = (row * D_ + nt * 64 + 8 * cb) * 2 - 1024 * i;
  }
}
template <int MODE>
DI void gemm_body(char* __restrict__ ws, char* smem, const u16* __restrict__ x, u16* __restrict__ out, int mt, int nt, int wv) {
  constexpr int NS = MODE ? 14 : 4, NBUF = 4, STG = 32768;
  const int lane = threadIdx.x & 63, r = lane & 31, h = lane >> 5;
  const int tbl = wv & 1, cbk = (wv >> 1) & 1, kh = wv >> 2;
  u32x4 rv[2];
  char* hp = ws + WS_H + ((mt * 2 + tbl) * 28 + nt * 4 + cbk * 2) * 1024 + lane * 16;
  if (kh == 0) {
    if (MODE) { rv[0] = *(const u32x4*)hp; rv[1] = *(const u32x4*)(hp + 1024); }
    else {
      const u16* rp = x + (size_t)(mt * 64 + tbl * 32 + r) * D_ + nt * 64 + cbk * 32 + 4 * h;
#pragma unroll
      for (int g = 0; g < 4; ++g) { u32x2 t = *(const u32x2*)(rp + 8 * g); rv[g >> 1][2 * (g & 1)] = t[0]; rv[g >> 1][2 * (g & 1) + 1] = t[1]; }
    }
  }
  f32x16 acc = {};
  const int q4 = (lane & 15) >> 2, p4 = lane & 3, blk = (lane >> 4) & 1;
  const int bcol = 32 * cbk + 16 * blk + 4 * p4;
  const int aoff = (MODE ? 0 : 32768) + tbl * 8192 + kh * 4096 + lane * 16;
  const int boff = (MODE ? 16384 : 0) + ((MODE ? 64 : 56) * kh + 4 * h + q4) * 128 + (((bcol >> 3) ^ ((q4 >> 1) << 2)) << 4) + (bcol & 7) * 2;
  struct Fr { u32x4 a[4], w[4]; } f0, f1;
  auto rd = [&](Fr& f, int so) {
#pragma unroll
    for (int ks = 0; ks < 4; ++ks) {
      f.a[ks] = lds_r128(smem + so + aoff + ks * 1024);
      const char* p = smem + so + boff + (MODE ? 16 * ks : 28 * (ks >> 1) + 16 * (ks & 1)) * 128;
      f.w[ks] = lds_tr2(p, p + 8 * 128);
    }
    SB();
  };
  auto mm = [&](const Fr& f) {
#pragma unroll
    for (int ks = 0; ks < 4; ++ks) acc = mfma_u(f.w[ks], f.a[ks], acc);
    SB();
  };
  if (MODE == 0) BAR();
  BAR();
  rd(f0, 0);
#pragma unroll 1
  for (int s = 0; s < NS; s += 2) {
    WAITL();
    BAR();
    rd(f1, MODE ? ((s + 1) & 3) * STG : s * 32768 + 16384); mm(f0);
    if (s + 2 < NS) { WAITL(); BAR(); rd(f0, MODE ? ((s + 2) & 3) * STG : 65536); }
    mm(f1);
  }
  char* fr = smem + (NS % NBUF) * STG;
  f32x4* red = (f32x4*)fr + (wv & 3) * 256 + lane;
  if (kh == 1) {
#pragma unroll
    for (int g = 0; g < 4; ++g) red[g * 64] = (f32x4){acc[4 * g], acc[4 * g + 1], acc[4 * g + 2], acc[4 * g + 3]};
  }
  WAITL();
  BAR();
  if (kh == 0) {
    unsigned u[8];
#pragma unroll
    for (int g = 0; g < 4; ++g) {
      const f32x4 t = red[g * 64];
      const unsigned t0 = pk2(acc[4 * g] + t[0], acc[4 * g + 1] + t[1]), t1 = pk2(acc[4 * g + 2] + t[2], acc[4 * g + 3] + t[3]);
      const unsigned r0 = rv[g >> 1][2 * (g & 1)], r1 = rv[g >> 1][2 * (g & 1) + 1];
      u[2 * g] = pk2(bf_lo(t0) + bf_lo(r0), bf_hi(t0) + bf_hi(r0));
      u[2 * g + 1] = pk2(bf_lo(t1) + bf_lo(r1), bf_hi(t1) + bf_hi(r1));
    }
    if (MODE == 0) {
      *(u32x4*)hp = (u32x4){u[0], u[1], u[2], u[3]};
      *(u32x4*)(hp + 1024) = (u32x4){u[4], u[5], u[6], u[7]};
      float s0 = 0.f, s1 = 0.f, q0 = 0.f, q1 = 0.f;
#pragma unroll
      for (int i = 0; i < 8; i += 2) {
        s0 = dot2(u[i], ONES2, s0); q0 = dot2(u[i], u[i], q0);
        s1 = dot2(u[i + 1], ONES2, s1); q1 = dot2(u[i + 1], u[i + 1], q1);
      }
      const f32x2 st = {sum32(s0 + s1), sum32(q0 + q1)};
      if (h == 0) ((f32x2*)(ws + WS_ST))[((mt * 2 + tbl) * 14 + nt * 2 + cbk) * 32 + r] = st;
    } else {
      char* tp = fr + 16384 + (tbl * 32 + r) * 136 + cbk * 64 + h * 8;
#pragma unroll
      for (int g = 0; g < 4; ++g) *(u32x2*)(tp + 16 * g) = (u32x2){u[2 * g], u[2 * g + 1]};
    }
  }
  if (MODE == 1) {
    WAITL();
    BAR();
    const int row = wv * 8 + (lane >> 3);
    const char* tp = fr + 16384 + row * 136 + (lane & 7) * 16;
    const u32x2 a = *(const u32x2*)tp, b = *(const u32x2*)(tp + 8);
    *(u32x4*)(out + (size_t)(mt * 64 + row) * D_ + nt * 64 + (lane & 7) * 8) = (u32x4){a[0], a[1], b[0], b[1]};
  }
}

constexpr int B_LDS = 4 * 32768 + 1024;

template <bool SAFE>
DI void attn_loop(char* smem, const u32x4 (&qf)[2], f32x16& o, float& m, int kh) {
  const int lane = threadIdx.x & 63;
  const f32x16 zero = {};
  BAR();
  const char* lp = smem + kh * 16384 + lane * 16;
  u32x4 kk[2][2], vv[4][2];
  f32x16 sc[2];
  kk[0][0] = lds_r128(lp); kk[0][1] = lds_r128(lp + 1024);
  kk[1][0] = lds_r128(lp + 4096); kk[1][1] = lds_r128(lp + 5120);
  vv[0][0] = lds_r128(lp + 2048); vv[0][1] = lds_r128(lp + 3072);
  vv[1][0] = lds_r128(lp + 6144); vv[1][1] = lds_r128(lp + 7168);
  sc[0] = mfma_u(kk[0][1], qf[1], mfma_u(kk[0][0], qf[0], zero));
  SB();
  if (SAFE) {
    u32x4 p0, p1 = {0u, 0u, 0u, 0u};
    vv[3][1] = p1;
#define EX2(d, i) d = pk2(__builtin_amdgcn_exp2f(cur[i]), __builtin_amdgcn_exp2f(cur[(i) + 1]))
#pragma unroll 1
    for (int s = 0; s < 8; ++s) {
#pragma unroll
      for (int b = 0; b < 4; ++b) {
        f32x16& cur = sc[b & 1];
        f32x16& nxt = sc[(b + 1) & 1];
        const char* np = lp + (b >= 2 ? ((s + 1) & 3) * 32768 + (b - 2) * 4096 : (s & 3) * 32768 + (b + 2) * 4096);
        nxt = mfma_u(kk[(b + 1) & 1][0], qf[0], zero);
        SB();
        if (b == 2 && s < 7) BAR();
        kk[b & 1][0] = lds_r128(np);
        EX2(p0[0], 0); EX2(p0[1], 2);
        SB();
        o = mfma_u(vv[(b + 3) & 3][1], p1, o);
        SB();
        kk[b & 1][1] = lds_r128(np + 1024);
        EX2(p0[2], 4); EX2(p0[3], 6);
        SB();
        nxt = mfma_u(kk[(b + 1) & 1][1], qf[1], nxt);
        SB();
        vv[(b + 2) & 3][0] = lds_r128(np + 2048);
        EX2(p1[0], 8); EX2(p1[1], 10);
        SB();
        o = mfma_u(vv[b][0], p0, o);
        SB();
        vv[(b + 2) & 3][1] = lds_r128(np + 3072);
        EX2(p1[2], 12); EX2(p1[3], 14);
        if (b == 1) WAITL();
        SB();
      }
    }
    o = mfma_u(vv[3][1], p1, o);
    return;
  }
#pragma unroll 1
  for (int s = 0; s < 8; ++s) {
#pragma unroll
    for (int b = 0; b < 4; ++b) {
      f32x16& cur = sc[b & 1];
      f32x16& nxt = sc[(b + 1) & 1];
      nxt = mfma_u(kk[(b + 1) & 1][0], qf[0], zero);
      SB();
      if (b == 2 && s < 7) {
        WAITL();
        BAR();
      }
      const char* np = lp + (b >= 2 ? ((s + 1) & 3) * 32768 + (b - 2) * 4096 : (s & 3) * 32768 + (b + 2) * 4096);
      kk[b & 1][0] = lds_r128(np); kk[b & 1][1] = lds_r128(np + 1024);
      vv[(b + 2) & 3][0] = lds_r128(np + 2048); vv[(b + 2) & 3][1] = lds_r128(np + 3072);
      SB();
      if (!SAFE) {
        float tm = cur[0];
#pragma unroll
        for (int i = 1; i < 16; ++i) tm = fmaxf(tm, cur[i]);
        const f32x2 t2 = halves(tm);
        const float mn = fmaxf(m, fmaxf(t2[0], t2[1]));
        const float al = __builtin_amdgcn_exp2f(m - mn);
        m = mn;
#pragma unroll
        for (int i = 0; i < 16; ++i) { o[i] *= al; cur[i] -= mn; }
      }
      u32x4 p0, p1;
#pragma unroll
      for (int i = 0; i < 4; ++i) p0[i] = pk2(__builtin_amdgcn_exp2f(cur[2 * i]), __builtin_amdgcn_exp2f(cur[2 * i + 1]));
      SB();
      nxt = mfma_u(kk[(b + 1) & 1][1], qf[1], nxt);
      o = mfma_u(vv[b][0], p0, o);
      SB();
#pragma unroll
      for (int i = 0; i < 4; ++i)
        p1[i] = pk2(__builtin_amdgcn_exp2f(cur[8 + 2 * i]), __builtin_amdgcn_exp2f(cur[8 + 2 * i + 1]));
      SB();
      o = mfma_u(vv[b][1], p1, o);
      SB();
    }
  }
}

__global__ __launch_bounds__(768) void k_attn(char* __restrict__ ws, const u16* __restrict__ wo, const u16* __restrict__ wg,
                                              const u16* __restrict__ wu, const u16* __restrict__ x) {
  extern __shared__ __attribute__((aligned(16))) char smem[];
  TSDECL
  const int lane = threadIdx.x & 63, wv = __builtin_amdgcn_readfirstlane(threadIdx.x >> 6);
  const int bid = blockIdx.x, xcd = bid & 7, idx = bid >> 3;
  const int mt = xcd * 4 + idx / 7, nt = idx % 7;
  unsigned* cnt = (unsigned*)(ws + WS_CNT);
  const int head = xcd * 2 + (idx >> 4), qt = idx & 15, qb = wv & 3, kh = wv >> 2, tb = qt * 4 + qb;
  if (wv >= 8) {
    const char* kv = ws + WS_KV + head * (64 * 4096);
    const int t = qb * 64 + lane, l16 = lane * 16;
    const unsigned junk = lds_base(smem) + 4 * 32768 + qb * 256;
    const int t2 = t < 196 ? t : 195;
    const int go = (idx * 14 + t2 / 14) * (F_ * 2) + (xcd & 1) * 1792 + (t2 % 14) * 128;
    u32x4 cw[4];
    ring_loader<8, 4, 32768, 8, 0, 2>([&](int s, int so) {
      const unsigned dst = lds_base(smem) + so + qb * 8192;
      const char* src = kv + ((qb >> 1) * 32 + s * 4) * 4096 + (qb & 1) * 8192;
      dma4(src, dst, l16, l16, l16, l16);
      dma4(src + 4096, dst + 4096, l16, l16, l16, l16);
      if (s == 7) { touch(wg, go, junk); touch(wu, go, junk); }
    }, [&](int s) {
      if (bid >= 196) return;
      const int rc = (bid / 28) * 4 + qb, cc = bid % 28;
      if (s == 4) {
        const u16* p = (cc < 14 ? wg : wu) + (rc * 16 + (lane >> 4)) * F_ + (cc < 14 ? cc : cc - 14) * 128 + (lane & 15) * 8;
#pragma unroll
        for (int i = 0; i < 4; ++i) cw[i] = *(const u32x4*)(p + 4 * i * F_);
      }
      if (s == 5) {
        float c[8];
#pragma unroll
        for (int i = 0; i < 4; ++i) {
          c[2 * i] = (bf_lo(cw[0][i]) + bf_lo(cw[1][i])) + (bf_lo(cw[2][i]) + bf_lo(cw[3][i]));
          c[2 * i + 1] = (bf_hi(cw[0][i]) + bf_hi(cw[1][i])) + (bf_hi(cw[2][i]) + bf_hi(cw[3][i]));
        }
#pragma unroll
        for (int i = 0; i < 8; ++i) { c[i] = sum32(c[i]); c[i] += __shfl_xor(c[i], 16); }
        f32x4* d = (f32x4*)(ws + WS_CS) + (rc * 28 + cc) * 32 + (lane & 15) * 2;
        if (lane < 16) { d[0] = (f32x4){c[0], c[1], c[2], c[3]}; d[1] = (f32x4){c[4], c[5], c[6], c[7]}; }
      }
    });
    if (idx < 28) {
      int bs[4];
      gemm_bsrc<0>(bs, qb, lane, nt);
#pragma unroll
      for (int s = 0; s < 4; ++s)
        dma4(wo + s * 112 * D_, lds_base(smem) + (s >> 1) * 65536 + (s & 1) * 16384 + qb * 4096, bs[0], bs[1], bs[2], bs[3]);
    }
    BAR();
    if (idx < 28) {
      BAR();
      spin_until(cnt + mt * 16, 32u);
      const char* A = ws + WS_O + (mt * 2 + (qb >> 1)) * 32768 + (qb & 1) * 4096;
#pragma unroll
      for (int s = 0; s < 4; ++s)
        dma4(A + s * 8192, lds_base(smem) + 32768 + (s >> 1) * 65536 + (s & 1) * 16384 + qb * 4096, l16, l16, l16, l16);
      WAITV(12); BAR();
      WAITV(8); BAR();
      WAITV(4); BAR();
      WAITV(0); BAR();
    }
    WAITV(0);
    return;
  }
  const char* qp = ws + WS_Q + (head * 64 + tb) * 2048 + lane * 16;
  const u32x4 qf[2] = {*(const u32x4*)qp, *(const u32x4*)(qp + 1024)};
  const unsigned* nn = (const unsigned*)(ws + WS_N) + head * 64 + lane;
  const unsigned nq = nn[0], k2 = wave_max_u(nn[1024]), q2 = wave_max_u((lane >> 2) == qt ? nq : 0u);
  const bool safe = __uint_as_float(k2) * __uint_as_float(q2) <= SAFE_T2;
  f32x16 o = {};
  float m = -3.0e38f;
  TS(2)
  if (safe) attn_loop<true>(smem, qf, o, m, kh);
  else attn_loop<false>(smem, qf, o, m, kh);
  TS(3)
  f32x4* red = (f32x4*)(smem + 32768) + qb * 320 + lane;
  if (kh == 1) {
#pragma unroll
    for (int g = 0; g < 4; ++g) red[g * 64] = (f32x4){o[4 * g], o[4 * g + 1], o[4 * g + 2], o[4 * g + 3]};
    ((float*)(red - lane + 256))[lane] = m;
  }
  WAITL();
  BAR();
  if (kh == 0) {
    float a0 = 1.f, a1 = 1.f;
    if (!safe) {
      const float m1 = ((const float*)(red - lane + 256))[lane], mm = fmaxf(m, m1);
      a0 = __builtin_amdgcn_exp2f(m - mm); a1 = __builtin_amdgcn_exp2f(m1 - mm);
    }
#pragma unroll
    for (int g = 0; g < 4; ++g) {
      const f32x4 t = red[g * 64];
#pragma unroll
      for (int i = 0; i < 4; ++i) o[4 * g + i] = o[4 * g + i] * a0 + t[i] * a1;
    }
    const float inv = 1.0f / halves(o[12])[1];
    unsigned u[8];
#pragma unroll
    for (int i = 0; i < 8; ++i) u[i] = pk2(o[2 * i] * inv, o[2 * i + 1] * inv);
    if (lane >= 32) { u[6] = 0u; u[7] = 0u; }
    char* dst = ws + WS_O + (tb * 16 + head) * 2048 + lane * 16;
    const u32x4 o0 = {u[0], u[1], u[2], u[3]}, o1 = {u[4], u[5], u[6], u[7]};
    asm volatile("global_store_dwordx4 %0, %1, off sc1\n\tglobal_store_dwordx4 %0, %2, off offset:1024 sc1\n\ts_waitcnt vmcnt(0)"
                 ::"v"(dst), "v"(o0), "v"(o1) : "memory");
    if (lane == 0) __hip_atomic_fetch_add(cnt + (tb >> 1) * 16, 1u, __ATOMIC_RELAXED, __HIP_MEMORY_SCOPE_AGENT);
  }
  TS(4)
  if (idx < 28) gemm_body<0>(ws, smem, x, nullptr, mt, nt, wv);
  TSEND(1)
}

constexpr int D_U = 16384 + 32, D_STG = 16384 + 2 * D_U, D_NBUF = 3, D_LDS = D_STG * D_NBUF + 1024;

__global__ __launch_bounds__(512) void k_ffn1(char* __restrict__ ws, const u16* __restrict__ wg, const u16* __restrict__ wu,
                                              const u16* __restrict__ wd) {
  extern __shared__ __attribute__((aligned(16))) char smem[];
  TSDECL
  const int wv = __builtin_amdgcn_readfirstlane(threadIdx.x >> 6), wq = wv & 3;
  const int bid = blockIdx.x, xcd = bid & 7, idx = bid >> 3;
  const int mt = (xcd >> 1) * 4 + idx / 7, nt = (xcd & 1) * 7 + idx % 7;
  const int lane = threadIdx.x & 63, r = lane & 31, h = lane >> 5;
  const int tb = mt * 4 + wq;
  float* cs = (float*)(smem + D_STG * D_NBUF);
  if (wv >= 4) {
    int src[4];
#pragma unroll
    for (int i = 0; i < 4; ++i)
      src[i] = ((wq & 1) * 32 + 4 * i + (lane >> 4)) * (F_ * 2) + nt * 256 + (((lane & 15) ^ ((lane >> 4) << 2)) << 4) - 1024 * i;
    const char* wsrc = (const char*)(wq < 2 ? wg : wu);
    const char* hsrc = ws + WS_H + tb * (28 * 1024);
    const int l16 = lane * 16, t = wq * 64 + lane;
    float cp[28];
    ring_loader<7, D_NBUF, D_STG, 12>([&](int s, int so) {
      const unsigned buf = lds_base(smem) + so + 16384 + (wq >> 1) * D_U + (wq & 1) * 8192;
      dma4(hsrc + s * 4096, lds_base(smem) + so + wq * 4096, l16, l16, l16, l16);
      dma4(wsrc + s * (64 * F_ * 2), buf, src[0], src[1], src[2], src[3]);
      dma4(wsrc + (s * 64 + 16) * (F_ * 2), buf + 4096, src[0], src[1], src[2], src[3]);
    }, [&](int s) {
      if (s == 3) {
        const float* p = (const float*)(ws + WS_CS) + ((t >> 4) & 1) * F_ + nt * 128 + (t >> 5) * 16 + (t & 15);
#pragma unroll
        for (int i = 0; i < 28; ++i) cp[i] = p[i * 2 * F_];
      }
      if (s == 5) {
        const unsigned junk = lds_base(smem) + (5 % D_NBUF) * D_STG + wq * 256;
        touch(wd, (idx * 448 + t) * 128, junk);
        touch(wd, (idx * 448 + 192 + t) * 128, junk);
        WAITV(0);
      }
      if (s == 4) {
        float c = 0.f;
#pragma unroll
        for (int i = 0; i < 28; ++i) c = fma1(cp[i], 1.0f, c);
        cs[t] = c;
      }
    });
    return;
  }
  const f32x2* sp = (const f32x2*)(ws + WS_ST) + (tb * 14 + 7 * h) * 32 + r;
  f32x2 ps[7];
#pragma unroll
  for (int i = 0; i < 7; ++i) ps[i] = sp[i * 32];
  const int q4 = (lane & 15) >> 2, p4 = lane & 3, blk = (lane >> 4) & 1;
  int bo[4];
#pragma unroll
  for (int i = 0; i < 4; ++i) bo[i] = 16384 + blk * D_U + (4 * h + q4) * 256 + ((i ^ q4) << 6) + 8 * p4;
  const char* ap = smem + wq * 4096 + lane * 16;
  f32x16 acc[8];
#pragma unroll
  for (int b = 0; b < 8; ++b)
#pragma unroll
    for (int i = 0; i < 16; ++i) acc[b][i] = 0.f;
  const f32x2 tot = ((ps[0] + ps[1]) + (ps[2] + ps[3])) + ((ps[4] + ps[5]) + ps[6]);
  f32x2 st = ln_scalars(sum32(tot[0]), sum32(tot[1]));
  PIN1(st);
  u32x4 w[2][8], a[2];
  SB();
  BAR();
  TS(2)
  a[0] = lds_r128(ap);
#pragma unroll
  for (int b = 0; b < 8; ++b) w[0][b] = lds_tr2(smem + bo[b >> 1] + (b & 1) * 32, smem + bo[b >> 1] + (b & 1) * 32 + 2048);
  SB();
  int so = 0;
#pragma unroll 1
  for (int s = 0; s < 7; ++s) {
#pragma unroll
    for (int j = 0; j < 4; ++j) {
      const int c = j & 1, n = c ^ 1, jn = (j + 1) & 3;
      __builtin_amdgcn_s_waitcnt(0xc07f);
      if (j == 3) {
        acc[0] = mfma_u(w[c][0], a[c], acc[0]);
        SB();
        if (s < 6) BAR();
        so = so == (D_NBUF - 1) * D_STG ? 0 : so + D_STG;
        SB();
      }
      const char* p = smem + so + jn * 4096;
      a[n] = lds_r128(ap + so + jn * 1024);
#pragma unroll
      for (int b = 0; b < 8; ++b) w[n][b] = lds_tr2(p + bo[b >> 1] + (b & 1) * 32, p + bo[b >> 1] + (b & 1) * 32 + 2048);
#pragma unroll
      for (int b = j == 3 ? 1 : 0; b < 8; ++b) acc[b] = mfma_u(w[c][b], a[c], acc[b]);
      if (j == 3) SG(0x100, 4);
#pragma unroll
      for (int b = 0; b < 5; ++b) { SG(0x008, 1); SG(0x100, 4); }
      SG(0x008, 3);
      SB();
    }
  }
  TS(9)
  const float k1 = st[1] * (-2.0f * 0.7978845608028654f * 1.4426950408889634f);
  const float k3 = k1 * st[1] * st[1] * 0.044715f, i2 = 1.0f / (st[1] * st[1]);
  const f32x2 c1 = {k1, k1}, c3 = {k3, k3}, ir2 = {i2, i2}, nm = {-st[0], -st[0]};
  char* dst = ws + WS_ACT + (size_t)(tb * 112 + nt * 8) * 1024 + lane * 16;
#pragma unroll
  for (int b = 0; b < 8; ++b) {
    u32x4 o;
    f32x4 c4[4];
#pragma unroll
    for (int g = 0; g < 4; ++g) c4[g] = *(const f32x4*)(cs + b * 32 + 8 * g + 4 * h);
#pragma unroll
    for (int i = 0; i < 4; ++i) {
      const int q = i >> 1, e = (i & 1) * 2;
      const f32x2 g = __builtin_elementwise_fma(nm, (f32x2){c4[q][e], c4[q][e + 1]}, (f32x2){acc[b][2 * i], acc[b][2 * i + 1]});
      const f32x2 u = __builtin_elementwise_fma(nm, (f32x2){c4[2 + q][e], c4[2 + q][e + 1]}, (f32x2){acc[b][8 + 2 * i], acc[b][9 + 2 * i]});
      const f32x2 z = __builtin_elementwise_fma(g * g, c3, c1) * g;
      const f32x2 d = __builtin_elementwise_fma((f32x2){__builtin_amdgcn_exp2f(z[0]), __builtin_amdgcn_exp2f(z[1])}, ir2, ir2);
      const f32x2 v = (g * u) * (f32x2){__builtin_amdgcn_rcpf(d[0]), __builtin_amdgcn_rcpf(d[1])};
      o[i] = pk2(v[0], v[1]);
    }
    *(u32x4*)(dst + b * 1024) = o;
  }
  TSEND(3)
}

constexpr int C_LDS = 4 * 32768;
__global__ __launch_bounds__(768) void k_down(char* __restrict__ ws, const u16* __restrict__ B, u16* __restrict__ out) {
  extern __shared__ __attribute__((aligned(16))) char smem[];
  TSDECL
  const int lane = threadIdx.x & 63, wv = __builtin_amdgcn_readfirstlane(threadIdx.x >> 6);
  const int bid = blockIdx.x, xcd = bid & 7, idx = bid >> 3;
  const int mt = xcd * 4 + idx / 7, nt = idx % 7;
  if (wv >= 8) {
    const int lw = wv - 8, l16 = lane * 16;
    const char* A = ws + WS_ACT + (size_t)(mt * 2 + (lw >> 1)) * 112 * 1024 + (lw & 1) * 4096;
    int bs[4];
    gemm_bsrc<1>(bs, lw, lane, nt);
    ring_loader<14, 4, 32768, 8>([&](int s, int so) {
      const unsigned buf = lds_base(smem) + so + lw * 4096;
      dma4(B + s * 128 * D_, buf + 16384, bs[0], bs[1], bs[2], bs[3]);
      dma4(A + s * 8192, buf, l16, l16, l16, l16);
    });
    return;
  }
  gemm_body<1>(ws, smem, nullptr, out, mt, nt, wv);
  TSEND(4)
}

extern "C" void kernel_launch(void* const* d_in, const int* in_sizes, int n_in, void* d_out, int out_size, void* d_ws,
                              size_t ws_size, hipStream_t stream) {
  const u16* x = (const u16*)d_in[0];
  const u16* wqkv = (const u16*)d_in[1];
  const u16* wo = (const u16*)d_in[2];
  const u16* wg = (const u16*)d_in[4];
  const u16* wu = (const u16*)d_in[5];
  const u16* wd = (const u16*)d_in[6];
  u16* out = (u16*)d_out;
  char* ws = (char*)d_ws;
  (void)hipFuncSetAttribute((const void*)k_qkv, hipFuncAttributeMaxDynamicSharedMemorySize, A_LDS);
  (void)hipFuncSetAttribute((const void*)k_attn, hipFuncAttributeMaxDynamicSharedMemorySize, B_LDS);
  (void)hipFuncSetAttribute((const void*)k_down, hipFuncAttributeMaxDynamicSharedMemorySize, C_LDS);
  (void)hipFuncSetAttribute((const void*)k_ffn1, hipFuncAttributeMaxDynamicSharedMemorySize, D_LDS);
  k_qkv<<<256, 768, A_LDS, stream>>>(x, wqkv, ws);
  k_attn<<<256, 768, B_LDS, stream>>>(ws, wo, wg, wu, x);
  k_ffn1<<<224, 512, D_LDS, stream>>>(ws, wg, wu, wd);
  k_down<<<224, 768, C_LDS, stream>>>(ws, wd, out);
}
```

```cpp
#include <hip/hip_runtime.h>
#include <hip/hip_bf16.h>

typedef unsigned short u16;
typedef __attribute__((ext_vector_type(8))) short bf16x8;
typedef __attribute__((ext_vector_type(4))) short s16x4;
typedef __attribute__((ext_vector_type(2))) __bf16 b16x2;
typedef __attribute__((ext_vector_type(2))) float f32x2;
typedef __attribute__((ext_vector_type(4))) float f32x4;
typedef __attribute__((ext_vector_type(16))) float f32x16;
typedef __attribute__((ext_vector_type(4))) unsigned u32x4;
typedef __attribute__((ext_vector_type(2))) unsigned u32x2;
#define LAS __attribute__((address_space(3)))
#define DI __device__ __forceinline__
#define WAITV(n) asm volatile("s_waitcnt vmcnt(%0) lgkmcnt(0)" ::"i"(n) : "memory")
#define WAITL() asm volatile("s_waitcnt lgkmcnt(0)" ::: "memory")
#define BAR() __builtin_amdgcn_s_barrier()
#define TSDECL
#define TS(i)
#define TSEND(kid)

__device__ __noinline__ void mark_lo() { asm volatile("s_nop 0"); }
__device__ __noinline__ void mark_hi();
constexpr int D_ = 448, DH_ = 28, F_ = 1792, N3_ = 1344;
constexpr float LN_EPS = 1e-5f;
constexpr float QSCALE = 0.18898223650461362f * 1.4426950408889634f;
constexpr float SAFE_T2 = 3600.0f;
constexpr unsigned WS_Q = 0, WS_KV = 2u << 20, WS_O = 6u << 20, WS_H = 8u << 20, WS_ACT = 10u << 20, WS_N = 18u << 20, WS_ST = 19u << 20, WS_CS = 20u << 20, WS_CNT = 21u << 20;

DI unsigned pk2(float lo, float hi) {
  f32x2 f = {lo, hi};
  return __builtin_bit_cast(unsigned, __builtin_convertvector(f, b16x2));
}
DI float bf_lo(unsigned u) { return __uint_as_float(u << 16); }
DI float bf_hi(unsigned u) { return __uint_as_float(u & 0xffff0000u); }
DI float rbf(float v) { return bf_lo(pk2(v, v)); }
DI float dot2(unsigned a, unsigned b, float c) {
  return __builtin_amdgcn_fdot2_f32_bf16(__builtin_bit_cast(b16x2, a), __builtin_bit_cast(b16x2, b), c, false);
}
DI float fma1(float a, float b, float c) {
  float d;
  asm("v_fma_f32 %0, %1, %2, %3" : "=v"(d) : "v"(a), "v"(b), "v"(c));
  return d;
}
constexpr unsigned ONES2 = 0x3f803f80u;
#define DMA_HEAD "s_mov_b32 %0, m0\n\ts_mov_b32 m0, %1\n\ts_nop 0\n\t"
#define DMA_L(v, o) "global_load_lds_dwordx4 %" #v ", %2 offset:" #o "\n\t"
#define DMA_TAIL "s_mov_b32 m0, %0"
DI void dma2(const void* sb, unsigned lds, int a, int b) {
  unsigned k;
  asm volatile(DMA_HEAD DMA_L(3, 0) DMA_L(4, 1024) DMA_TAIL : "=&s"(k) : "s"(lds), "s"(sb), "v"(a), "v"(b) : "memory");
}
DI void dma3(const void* sb, unsigned lds, int a, int b, int c) {
  unsigned k;
  asm volatile(DMA_HEAD DMA_L(3, 0) DMA_L(4, 1024) DMA_L(5, 2048) DMA_TAIL
               : "=&s"(k) : "s"(lds), "s"(sb), "v"(a), "v"(b), "v"(c) : "memory");
}
DI void dma4(const void* sb, unsigned lds, int a, int b, int c, int d) {
  unsigned k;
  asm volatile(DMA_HEAD DMA_L(3, 0) DMA_L(4, 1024) DMA_L(5, 2048) DMA_L(6, 3072) DMA_TAIL
               : "=&s"(k) : "s"(lds), "s"(sb), "v"(a), "v"(b), "v"(c), "v"(d) : "memory");
}
DI void touch(const void* sb, int voff, unsigned lds) {
  unsigned k;
  asm volatile(DMA_HEAD "global_load_lds_dword %3, %2\n\t" DMA_TAIL : "=&s"(k) : "s"(lds), "s"(sb), "v"(voff) : "memory");
}
#define SB() __builtin_amdgcn_sched_barrier(0)
#define SG(m, n) __builtin_amdgcn_sched_group_barrier(m, n, 0)
#define PIN2(a, b) asm volatile("" : "+v"(a), "+v"(b))
#define PIN1(a) asm volatile("" : "+v"(a))
DI unsigned lds_base(char* smem) { return (unsigned)(size_t)(LAS char*)smem; }
DI u32x4 lds_r128(const char* p) { return *(const u32x4*)p; }
DI u32x4 lds_tr2(const char* p0, const char* p1) {
  s16x4 a = __builtin_amdgcn_ds_read_tr16_b64_v4i16((LAS s16x4*)p0);
  s16x4 b = __builtin_amdgcn_ds_read_tr16_b64_v4i16((LAS s16x4*)p1);
  u32x2 ua = __builtin_bit_cast(u32x2, a), ub = __builtin_bit_cast(u32x2, b);
  u32x4 r = {ua[0], ua[1], ub[0], ub[1]};
  return r;
}
DI f32x16 mfma_u(u32x4 a, u32x4 b, f32x16 c) {
  return __builtin_amdgcn_mfma_f32_32x32x16_bf16(__builtin_bit_cast(bf16x8, a), __builtin_bit_cast(bf16x8, b), c, 0, 0, 0);
}
DI f32x2 halves(float v) {
  auto r = __builtin_amdgcn_permlane32_swap(__float_as_uint(v), __float_as_uint(v), false, false);
  return (f32x2){__uint_as_float(r[0]), __uint_as_float(r[1])};
}
DI float sum32(float v) { f32x2 t = halves(v); return t[0] + t[1]; }
DI unsigned wave_max_u(unsigned u) {
  u = max(u, (unsigned)__builtin_amdgcn_update_dpp(0, (int)u, 0x128, 0xf, 0xf, false));
  u = max(u, (unsigned)__builtin_amdgcn_update_dpp(0, (int)u, 0x124, 0xf, 0xf, false));
  u = max(u, (unsigned)__builtin_amdgcn_update_dpp(0, (int)u, 0x122, 0xf, 0xf, false));
  u = max(u, (unsigned)__builtin_amdgcn_update_dpp(0, (int)u, 0x121, 0xf, 0xf, false));
  unsigned a = __builtin_amdgcn_readlane(u, 0), b = __builtin_amdgcn_readlane(u, 16);
  unsigned c = __builtin_amdgcn_readlane(u, 32), d = __builtin_amdgcn_readlane(u, 48);
  return max(max(a, b), max(c, d));
}
DI void spin_until(unsigned* c, unsigned n) {
  while (__hip_atomic_load(c, __ATOMIC_RELAXED, __HIP_MEMORY_SCOPE_AGENT) < n) __builtin_amdgcn_s_sleep(2);
}
DI f32x2 ln_scalars(float sum, float sq) {
  const float mean = sum * (1.0f / D_);
  float var = sq * (1.0f / D_) - mean * mean;
  var = var > 0.f ? var : 0.f;
  const float sd = rbf(sqrtf(rbf(rbf(var) + LN_EPS)));
  return (f32x2){rbf(mean), 1.0f / sd};
}

template <int ND, int E4, int E7>
constexpr int ring_allow(int k, int last) {
  int n = (last - k) * ND;
  if (k <= 4 && last >= 4) n += E4;
  if (k <= 7 && last >= 7) n += E7;
  return n;
}
#define RP 2
#define RR 2
template <int NS, int NBUF>
constexpr int ring_cnt(int s) {
  int n = RP + (NBUF == 5 ? 1 : RR) * (s + 1);
  n = n < s + NBUF + (s < 0) ? n : s + NBUF + (s < 0);
  return n < NS ? n : NS;
}
struct NoHook { DI void operator()(int) const {} };
template <int NS, int NBUF, int STG, int ND, int E4 = 0, int E7 = 0, class F, class H = NoHook>
DI void ring_loader(F issue, H idle = H()) {
#pragma unroll
  for (int s = -1; s < NS - 1; ++s) {
#pragma unroll
    for (int k = s < 0 ? 0 : ring_cnt<NS, NBUF>(s - 1); k < ring_cnt<NS, NBUF>(s); ++k) issue(k, (k % NBUF) * STG);
    if (s > 0) idle(s - 1);
    WAITV((ring_allow<ND, E4, E7>(s + 1, ring_cnt<NS, NBUF>(s) - 1)));
    BAR();
  }
  idle(NS - 2);
}

constexpr int A_XB = 128 * 128, A_WB = 64 * 192, A_STG = A_XB + A_WB, A_NBUF = 5;
constexpr int A_MISC = A_STG * A_NBUF;
constexpr int A_LDS = A_MISC + 5 * 96 * 4 + 3 * 128 * 8 + 256;
#define TSLEEP 20

template <int GRP, bool ISV>
DI void qkv_body(char* __restrict__ ws, char* smem, int mt, int nt, int wq) {
  TSDECL
  constexpr int NB = GRP == 0 ? 2 : 1, B0 = GRP == 0 ? 0 : 2;
  const int lane = threadIdx.x & 63, r = lane & 31, h = lane >> 5;
  const int off = (84 * nt) & 7;
  f32x16 acc[NB];
  float cs[NB];
#pragma unroll
  for (int b = 0; b < NB; ++b) {
    cs[b] = 0.f;
#pragma unroll
    for (int i = 0; i < 16; ++i) acc[b][i] = 0.f;
  }
  float sum = 0.f, sq = 0.f, sum1 = 0.f, sq1 = 0.f;
  const int xrow = 32 * wq + r, xsw = (xrow >> 1) & 7;
  const int q4 = (lane & 15) >> 2, p4 = lane & 3, blk = (lane >> 4) & 1;
  int ao[4], bo[4];
#pragma unroll
  for (int p = 0; p < 4; ++p) {
    const int kk = (p + wq) & 3;
    ao[p] = xrow * 128 + (((2 * kk + h) ^ xsw) << 4);
    bo[p] = A_XB + (16 * kk + 8 * h + q4) * 192 + (off + 16 * blk + 4 * p4) * 2 + B0 * 56;
  }
  struct Fr { u32x4 a[4], w[4][NB]; } f0, f1;
  auto rd = [&](Fr& f, int so) {
#pragma unroll
    for (int p = 0; p < 4; ++p) {
      f.a[p] = lds_r128(smem + so + ao[p]);
#pragma unroll
      for (int b = 0; b < NB; ++b) f.w[p][b] = lds_tr2(smem + so + bo[p] + b * 56, smem + so + bo[p] + b * 56 + 4 * 192);
    }
  };
  auto mm = [&](const Fr& f) {
#pragma unroll
    for (int p = 0; p < 4; ++p)
#pragma unroll
      for (int b = 0; b < NB; ++b) acc[b] = ISV ? mfma_u(f.a[p], f.w[p][b], acc[b]) : mfma_u(f.w[p][b], f.a[p], acc[b]);
#pragma unroll
    for (int i = 0; i < 4 * NB; ++i) { SG(0x008, 1); SG(0x100, 3); }
    SB();
#pragma unroll
    for (int p = GRP; p < (GRP ? 4 : 1); ++p) {
#pragma unroll
      for (int i = 0; i < 4; i += 2) {
        sum = dot2(f.a[p][i], ONES2, sum); sq = dot2(f.a[p][i], f.a[p][i], sq);
        sum1 = dot2(f.a[p][i + 1], ONES2, sum1); sq1 = dot2(f.a[p][i + 1], f.a[p][i + 1], sq1);
      }
    }
    PIN2(sum, sq); PIN2(sum1, sq1);
#pragma unroll
    for (int i = 0; i < 4; ++i)
#pragma unroll
      for (int b = 0; b < NB; ++b) cs[b] = dot2(f.w[0][b][i], ONES2, cs[b]);
#pragma unroll
    for (int b = 0; b < NB; ++b) PIN1(cs[b]);
    SB();
  };
  BAR();
  TS(2)
  rd(f0, 0);
  SB();
  int so = 0;
#pragma unroll 1
  for (int s = 0; s < 6; s += 2) {
    __builtin_amdgcn_s_waitcnt(0xc07f);
    BAR();
    SB();
    so = so == (A_NBUF - 1) * A_STG ? 0 : so + A_STG;
    rd(f1, so); mm(f0);
    __builtin_amdgcn_s_waitcnt(0xc07f);
    BAR();
    SB();
    so = so == (A_NBUF - 1) * A_STG ? 0 : so + A_STG;
    rd(f0, so); mm(f1);
  }
  mm(f0);
  TS(9)
  float* csp = (float*)(smem + A_MISC);
  f32x2* tst = (f32x2*)(smem + A_MISC + 5 * 96 * 4);
#pragma unroll
  for (int b = 0; b < NB; ++b) {
    const float c = sum32(cs[b]);
    if (h == 0) csp[wq * 96 + (B0 + b) * 32 + r] = c;
  }
  {
    const f32x2 pr = {sum32(sum + sum1), sum32(sq + sq1)};
    if (h == 0) tst[128 + GRP * 128 + 32 * wq + r] = pr;
  }
  WAITL();
  BAR();
  BAR();
  const float* ct = csp + 384;
  const int tb = mt * 4 + wq;
#pragma unroll
  for (int b = 0; b < NB; ++b) {
    const int gb = 3 * nt + B0 + b, head = gb & 15, type = gb >> 4, cb = (B0 + b) * 32;
    unsigned o[8];
    if (ISV) {
      const float c = ct[cb + r];
#pragma unroll
      for (int g = 0; g < 4; ++g) {
        const float* sp = (const float*)(tst + 32 * wq + 8 * g + 4 * h);
        f32x4 s0 = *(const f32x4*)sp, s1 = *(const f32x4*)(sp + 4);
        float v0 = s0[1] * (acc[b][4 * g] - s0[0] * c), v1 = s0[3] * (acc[b][4 * g + 1] - s0[2] * c);
        float v2 = s1[1] * (acc[b][4 * g + 2] - s1[0] * c), v3 = s1[3] * (acc[b][4 * g + 3] - s1[2] * c);
        o[2 * g] = pk2(v0, v1); o[2 * g + 1] = pk2(v2, v3);
      }
      if (r >= DH_) {
        const unsigned f = (r == DH_) ? ONES2 : 0u;
#pragma unroll
        for (int i = 0; i < 8; ++i) o[i] = f;
      }
      char* dst = ws + WS_KV + (head * 64 + tb) * 4096 + 2048 + lane * 16;
      *(u32x4*)dst = (u32x4){o[0], o[1], o[2], o[3]};
      *(u32x4*)(dst + 1024) = (u32x4){o[4], o[5], o[6], o[7]};
    } else {
      const f32x2 st = tst[32 * wq + r];
      const float sc = type == 0 ? st[1] * QSCALE : st[1];
      float n2 = 0.f;
#pragma unroll
      for (int g = 0; g < 4; ++g) {
        const f32x4 c4 = *(const f32x4*)(ct + cb + 8 * g + 4 * h);
        float v0 = sc * (acc[b][4 * g] - st[0] * c4[0]), v1 = sc * (acc[b][4 * g + 1] - st[0] * c4[1]);
        float v2 = sc * (acc[b][4 * g + 2] - st[0] * c4[2]), v3 = sc * (acc[b][4 * g + 3] - st[0] * c4[3]);
        unsigned a = pk2(v0, v1), bb = pk2(v2, v3);
        if (g == 3 && h == 1) { a = 0u; bb = 0u; }
        o[2 * g] = a; o[2 * g + 1] = bb;
        n2 = dot2(a, a, n2); n2 = dot2(bb, bb, n2);
      }
      char* dst = ws + (type == 0 ? WS_Q + (head * 64 + tb) * 2048 : WS_KV + (head * 64 + tb) * 4096) + lane * 16;
      *(u32x4*)dst = (u32x4){o[0], o[1], o[2], o[3]};
      *(u32x4*)(dst + 1024) = (u32x4){o[4], o[5], o[6], o[7]};
      const unsigned mx = wave_max_u(__float_as_uint(sum32(n2)));
      if (lane == 0) ((unsigned*)(ws + WS_N))[type * 1024 + head * 64 + tb] = mx;
    }
  }
  TSEND(0)
}

__global__ __launch_bounds__(768) void k_qkv(const u16* __restrict__ x, const u16* __restrict__ w, char* __restrict__ ws) {
  extern __shared__ __attribute__((aligned(16))) char smem[];
  const int bid = blockIdx.x, xcd = bid & 7, idx = bid >> 3;
  const int mt = (xcd >> 1) * 4 + (idx >> 3), nt = (xcd & 1) * 8 + (idx & 7);
  const int wv = __builtin_amdgcn_readfirstlane(threadIdx.x >> 6), wq = wv & 3;
  if (wv >= 8) {
    const int lane = threadIdx.x & 63, t = wq * 64 + lane;
    if (bid == 0 && t < 32) ((unsigned*)(ws + WS_CNT))[t * 16] = 0u;
    const int ca = (84 * nt) & ~7;
    int sw[3], sx[4];
#pragma unroll
    for (int i = 0; i < 3; ++i) {
      int ci = 64 * (3 * wq + i) + lane, row = ci / 12, c = ci - row * 12;
      int col = ca + 8 * c;
      col = col > (N3_ - 8) ? (N3_ - 8) : col;
      sw[i] = (row * N3_ + col) * 2 - 1024 * i;
    }
#pragma unroll
    for (int i = 0; i < 4; ++i) {
      int c = (lane & 7) ^ (((i & 1) * 4 + (lane >> 4)) & 7);
      sx[i] = ((mt * 128 + 32 * wq + 8 * i + (lane >> 3)) * D_ + 8 * c) * 2 - 1024 * i;
    }
    ring_loader<7, A_NBUF, A_STG, 7>([&](int s, int so) {
      dma3(w + 64 * s * N3_, lds_base(smem) + so + A_XB + 3 * wq * 1024, sw[0], sw[1], sw[2]);
      dma4(x + 64 * s, lds_base(smem) + so + wq * 4096, sx[0], sx[1], sx[2], sx[3]);
    });
    BAR();
    float* csp = (float*)(smem + A_MISC);
    f32x2* tst = (f32x2*)(smem + A_MISC + 5 * 96 * 4);
    if (t < 96) csp[384 + t] = csp[t] + csp[96 + t] + csp[192 + t] + csp[288 + t];
    if (t >= 128) {
      const f32x2 p = tst[t] + tst[t + 128];
      tst[t - 128] = ln_scalars(p[0], p[1]);
    }
    WAITL();
    BAR();
    return;
  }
  if (wv < 5) {
    __builtin_amdgcn_s_sleep(TSLEEP);
    const int t = threadIdx.x;
    const unsigned junk = lds_base(smem) + A_LDS - 256;
    const int t1 = t < 112 ? t : 111, t2 = t - 128 < 154 ? t - 128 : 153;
    if (wv < 2) touch(x, ((xcd >> 1) * 512 + idx * 16) * (D_ * 2) + t1 * 128, junk);
    else touch(w, (idx * 14 + t2 / 11) * (N3_ * 2) + ((xcd & 1) * 10 + t2 % 11) * 128, junk);
  }
  if (wv == 5) {
    const char *c0 = (const char*)&mark_lo, *c1 = (const char*)&mark_hi;
    const char* lo = c0 < c1 ? c0 : c1;
    const int len = (int)(c0 < c1 ? c1 - c0 : c0 - c1);
    int off = (idx * 32 + (threadIdx.x & 31)) * 128;
    off = off + 4 <= len ? off : 0;
    touch(lo, off, lds_base(smem) + A_LDS - 256);
  }
  if (wv < 4) {
    if (nt >= 11) qkv_body<0, true>(ws, smem, mt, nt, wq);
    else qkv_body<0, false>(ws, smem, mt, nt, wq);
  } else {
    if (nt >= 10) qkv_body<1, true>(ws, smem, mt, nt, wq);
    else qkv_body<1, false>(ws, smem, mt, nt, wq);
  }
}

template <int MODE>
DI void gemm_bsrc(int (&bs)[4], int lw, int lane, int nt) {
#pragma unroll
  for (int i = 0; i < 4; ++i) {
    int row = 32 * lw + 8 * i + (lane >> 3);
    const int cb = (lane & 7) ^ (((row >> 1) & 1) << 2);
    if (MODE == 0 && row >= 112) row -= 16;
    bs[i] = (row * D_ + nt * 64 + 8 * cb) * 2 - 1024 * i;
  }
}
template <int MODE>
DI void gemm_body(char* __restrict__ ws, char* smem, const u16* __restrict__ x, u16* __restrict__ out, int mt, int nt, int wv) {
  constexpr int NS = MODE ? 14 : 4, NBUF = 4, STG = 32768;
  const int lane = threadIdx.x & 63, r = lane & 31, h = lane >> 5;
  const int tbl = wv & 1, cbk = (wv >> 1) & 1, kh = wv >> 2;
  u32x4 rv[2];
  char* hp = ws + WS_H + ((mt * 2 + tbl) * 28 + nt * 4 + cbk * 2) * 1024 + lane * 16;
  if (kh == 0) {
    if (MODE) { rv[0] = *(const u32x4*)hp; rv[1] = *(const u32x4*)(hp + 1024); }
    else {
      const u16* rp = x + (size_t)(mt * 64 + tbl * 32 + r) * D_ + nt * 64 + cbk * 32 + 4 * h;
#pragma unroll
      for (int g = 0; g < 4; ++g) { u32x2 t = *(const u32x2*)(rp + 8 * g); rv[g >> 1][2 * (g & 1)] = t[0]; rv[g >> 1][2 * (g & 1) + 1] = t[1]; }
    }
  }
  f32x16 acc = {};
  const int q4 = (lane & 15) >> 2, p4 = lane & 3, blk = (lane >> 4) & 1;
  const int bcol = 32 * cbk + 16 * blk + 4 * p4;
  const int aoff = (MODE ? 0 : 32768) + tbl * 8192 + kh * 4096 + lane * 16;
  const int boff = (MODE ? 16384 : 0) + ((MODE ? 64 : 56) * kh + 4 * h + q4) * 128 + (((bcol >> 3) ^ ((q4 >> 1) << 2)) << 4) + (bcol & 7) * 2;
  struct Fr { u32x4 a[4], w[4]; } f0, f1;
  auto rd = [&](Fr& f, int so) {
#pragma unroll
    for (int ks = 0; ks < 4; ++ks) {
      f.a[ks] = lds_r128(smem + so + aoff + ks * 1024);
      const char* p = smem + so + boff + (MODE ? 16 * ks : 28 * (ks >> 1) + 16 * (ks & 1)) * 128;
      f.w[ks] = lds_tr2(p, p + 8 * 128);
    }
    SB();
  };
  auto mm = [&](const Fr& f) {
#pragma unroll
    for (int ks = 0; ks < 4; ++ks) acc = mfma_u(f.w[ks], f.a[ks], acc);
    SB();
  };
  if (MODE == 0) BAR();
  BAR();
  rd(f0, 0);
#pragma unroll 1
  for (int s = 0; s < NS; s += 2) {
    WAITL();
    BAR();
    rd(f1, MODE ? ((s + 1) & 3) * STG : s * 32768 + 16384); mm(f0);
    if (s + 2 < NS) { WAITL(); BAR(); rd(f0, MODE ? ((s + 2) & 3) * STG : 65536); }
    mm(f1);
  }
  char* fr = smem + (NS % NBUF) * STG;
  f32x4* red = (f32x4*)fr + (wv & 3) * 256 + lane;
  if (kh == 1) {
#pragma unroll
    for (int g = 0; g < 4; ++g) red[g * 64] = (f32x4){acc[4 * g], acc[4 * g + 1], acc[4 * g + 2], acc[4 * g + 3]};
  }
  WAITL();
  BAR();
  if (kh == 0) {
    unsigned u[8];
#pragma unroll
    for (int g = 0; g < 4; ++g) {
      const f32x4 t = red[g * 64];
      const unsigned t0 = pk2(acc[4 * g] + t[0], acc[4 * g + 1] + t[1]), t1 = pk2(acc[4 * g + 2] + t[2], acc[4 * g + 3] + t[3]);
      const unsigned r0 = rv[g >> 1][2 * (g & 1)], r1 = rv[g >> 1][2 * (g & 1) + 1];
      u[2 * g] = pk2(bf_lo(t0) + bf_lo(r0), bf_hi(t0) + bf_hi(r0));
      u[2 * g + 1] = pk2(bf_lo(t1) + bf_lo(r1), bf_hi(t1) + bf_hi(r1));
    }
    if (MODE == 0) {
      *(u32x4*)hp = (u32x4){u[0], u[1], u[2], u[3]};
      *(u32x4*)(hp + 1024) = (u32x4){u[4], u[5], u[6], u[7]};
      float s0 = 0.f, s1 = 0.f, q0 = 0.f, q1 = 0.f;
#pragma unroll
      for (int i = 0; i < 8; i += 2) {
        s0 = dot2(u[i], ONES2, s0); q0 = dot2(u[i], u[i], q0);
        s1 = dot2(u[i + 1], ONES2, s1); q1 = dot2(u[i + 1], u[i + 1], q1);
      }
      const f32x2 st = {sum32(s0 + s1), sum32(q0 + q1)};
      if (h == 0) ((f32x2*)(ws + WS_ST))[((mt * 2 + tbl) * 14 + nt * 2 + cbk) * 32 + r] = st;
    } else {
      char* tp = fr + 16384 + (tbl * 32 + r) * 136 + cbk * 64 + h * 8;
#pragma unroll
      for (int g = 0; g < 4; ++g) *(u32x2*)(tp + 16 * g) = (u32x2){u[2 * g], u[2 * g + 1]};
    }
  }
  if (MODE == 1) {
    WAITL();
    BAR();
    const int row = wv * 8 + (lane >> 3);
    const char* tp = fr + 16384 + row * 136 + (lane & 7) * 16;
    const u32x2 a = *(const u32x2*)tp, b = *(const u32x2*)(tp + 8);
    *(u32x4*)(out + (size_t)(mt * 64 + row) * D_ + nt * 64 + (lane & 7) * 8) = (u32x4){a[0], a[1], b[0], b[1]};
  }
}

constexpr int B_LDS = 4 * 32768 + 1024;

template <bool SAFE>
DI void attn_loop(char* smem, const u32x4 (&qf)[2], f32x16& o, float& m, int kh) {
  const int lane = threadIdx.x & 63;
  const f32x16 zero = {};
  BAR();
  const char* lp = smem + kh * 16384 + lane * 16;
  u32x4 kk[2][2], vv[4][2];
  f32x16 sc[2];
  kk[0][0] = lds_r128(lp); kk[0][1] = lds_r128(lp + 1024);
  kk[1][0] = lds_r128(lp + 4096); kk[1][1] = lds_r128(lp + 5120);
  vv[0][0] = lds_r128(lp + 2048); vv[0][1] = lds_r128(lp + 3072);
  vv[1][0] = lds_r128(lp + 6144); vv[1][1] = lds_r128(lp + 7168);
  sc[0] = mfma_u(kk[0][1], qf[1], mfma_u(kk[0][0], qf[0], zero));
  SB();
  if (SAFE) {
    u32x4 p0, p1 = {0u, 0u, 0u, 0u};
    vv[3][1] = p1;
#define EX2(d, i) d = pk2(__builtin_amdgcn_exp2f(cur[i]), __builtin_amdgcn_exp2f(cur[(i) + 1]))
#pragma unroll 1
    for (int s = 0; s < 8; ++s) {
#pragma unroll
      for (int b = 0; b < 4; ++b) {
        f32x16& cur = sc[b & 1];
        f32x16& nxt = sc[(b + 1) & 1];
        const char* np = lp + (b >= 2 ? ((s + 1) & 3) * 32768 + (b - 2) * 4096 : (s & 3) * 32768 + (b + 2) * 4096);
        nxt = mfma_u(kk[(b + 1) & 1][0], qf[0], zero);
        SB();
        if (b == 2 && s < 7) BAR();
        kk[b & 1][0] = lds_r128(np);
        EX2(p0[0], 0); EX2(p0[1], 2);
        SB();
        o = mfma_u(vv[(b + 3) & 3][1], p1, o);
        SB();
        kk[b & 1][1] = lds_r128(np + 1024);
        EX2(p0[2], 4); EX2(p0[3], 6);
        SB();
        nxt = mfma_u(kk[(b + 1) & 1][1], qf[1], nxt);
        SB();
        vv[(b + 2) & 3][0] = lds_r128(np + 2048);
        EX2(p1[0], 8); EX2(p1[1], 10);
        SB();
        o = mfma_u(vv[b][0], p0, o);
        SB();
        vv[(b + 2) & 3][1] = lds_r128(np + 3072);
        EX2(p1[2], 12); EX2(p1[3], 14);
        if (b == 1) WAITL();
        SB();
      }
    }
    o = mfma_u(vv[3][1], p1, o);
    return;
  }
#pragma unroll 1
  for (int s = 0; s < 8; ++s) {
#pragma unroll
    for (int b = 0; b < 4; ++b) {
      f32x16& cur = sc[b & 1];
      f32x16& nxt = sc[(b + 1) & 1];
      nxt = mfma_u(kk[(b + 1) & 1][0], qf[0], zero);
      SB();
      if (b == 2 && s < 7) {
        WAITL();
        BAR();
      }
      const char* np = lp + (b >= 2 ? ((s + 1) & 3) * 32768 + (b - 2) * 4096 : (s & 3) * 32768 + (b + 2) * 4096);
      kk[b & 1][0] = lds_r128(np); kk[b & 1][1] = lds_r128(np + 1024);
      vv[(b + 2) & 3][0] = lds_r128(np + 2048); vv[(b + 2) & 3][1] = lds_r128(np + 3072);
      SB();
      if (!SAFE) {
        float tm = cur[0];
#pragma unroll
        for (int i = 1; i < 16; ++i) tm = fmaxf(tm, cur[i]);
        const f32x2 t2 = halves(tm);
        const float mn = fmaxf(m, fmaxf(t2[0], t2[1]));
        const float al = __builtin_amdgcn_exp2f(m - mn);
        m = mn;
#pragma unroll
        for (int i = 0; i < 16; ++i) { o[i] *= al; cur[i] -= mn; }
      }
      u32x4 p0, p1;
#pragma unroll
      for (int i = 0; i < 4; ++i) p0[i] = pk2(__builtin_amdgcn_exp2f(cur[2 * i]), __builtin_amdgcn_exp2f(cur[2 * i + 1]));
      SB();
      nxt = mfma_u(kk[(b + 1) & 1][1], qf[1], nxt);
      o = mfma_u(vv[b][0], p0, o);
      SB();
#pragma unroll
      for (int i = 0; i < 4; ++i)
        p1[i] = pk2(__builtin_amdgcn_exp2f(cur[8 + 2 * i]), __builtin_amdgcn_exp2f(cur[8 + 2 * i + 1]));
      SB();
      o = mfma_u(vv[b][1], p1, o);
      SB();
    }
  }
}

__global__ __launch_bounds__(768) void k_attn(char* __restrict__ ws, const u16* __restrict__ wo, const u16* __restrict__ wg,
                                              const u16* __restrict__ wu, const u16* __restrict__ x) {
  extern __shared__ __attribute__((aligned(16))) char smem[];
  TSDECL
  const int lane = threadIdx.x & 63, wv = __builtin_amdgcn_readfirstlane(threadIdx.x >> 6);
  const int bid = blockIdx.x, xcd = bid & 7, idx = bid >> 3;
  const int mt = xcd * 4 + idx / 7, nt = idx % 7;
  unsigned* cnt = (unsigned*)(ws + WS_CNT);
  const int head = xcd * 2 + (idx >> 4), qt = idx & 15, qb = wv & 3, kh = wv >> 2, tb = qt * 4 + qb;
  if (wv >= 8) {
    const char* kv = ws + WS_KV + head * (64 * 4096);
    const int t = qb * 64 + lane, l16 = lane * 16;
    const unsigned junk = lds_base(smem) + 4 * 32768 + qb * 256;
    const int t2 = t < 196 ? t : 195;
    const int go = (idx * 14 + t2 / 14) * (F_ * 2) + (xcd & 1) * 1792 + (t2 % 14) * 128;
    u32x4 cw[4];
    ring_loader<8, 4, 32768, 8, 0, 2>([&](int s, int so) {
      const unsigned dst = lds_base(smem) + so + qb * 8192;
      const char* src = kv + ((qb >> 1) * 32 + s * 4) * 4096 + (qb & 1) * 8192;
      dma4(src, dst, l16, l16, l16, l16);
      dma4(src + 4096, dst + 4096, l16, l16, l16, l16);
      if (s == 7) { touch(wg, go, junk); touch(wu, go, junk); }
    }, [&](int s) {
      if (bid >= 196) return;
      const int rc = (bid / 28) * 4 + qb, cc = bid % 28;
      if (s == 4) {
        const u16* p = (cc < 14 ? wg : wu) + (rc * 16 + (lane >> 4)) * F_ + (cc < 14 ? cc : cc - 14) * 128 + (lane & 15) * 8;
#pragma unroll
        for (int i = 0; i < 4; ++i) cw[i] = *(const u32x4*)(p + 4 * i * F_);
      }
      if (s == 5) {
        float c[8];
#pragma unroll
        for (int i = 0; i < 4; ++i) {
          c[2 * i] = (bf_lo(cw[0][i]) + bf_lo(cw[1][i])) + (bf_lo(cw[2][i]) + bf_lo(cw[3][i]));
          c[2 * i + 1] = (bf_hi(cw[0][i]) + bf_hi(cw[1][i])) + (bf_hi(cw[2][i]) + bf_hi(cw[3][i]));
        }
#pragma unroll
        for (int i = 0; i < 8; ++i) { c[i] = sum32(c[i]); c[i] += __shfl_xor(c[i], 16); }
        f32x4* d = (f32x4*)(ws + WS_CS) + (rc * 28 + cc) * 32 + (lane & 15) * 2;
        if (lane < 16) { d[0] = (f32x4){c[0], c[1], c[2], c[3]}; d[1] = (f32x4){c[4], c[5], c[6], c[7]}; }
      }
    });
    if (idx < 28) {
      int bs[4];
      gemm_bsrc<0>(bs, qb, lane, nt);
#pragma unroll
      for (int s = 0; s < 4; ++s)
        dma4(wo + s * 112 * D_, lds_base(smem) + (s >> 1) * 65536 + (s & 1) * 16384 + qb * 4096, bs[0], bs[1], bs[2], bs[3]);
    }
    BAR();
    if (idx < 28) {
      BAR();
      spin_until(cnt + mt * 16, 32u);
      const char* A = ws + WS_O + (mt * 2 + (qb >> 1)) * 32768 + (qb & 1) * 4096;
#pragma unroll
      for (int s = 0; s < 4; ++s)
        dma4(A + s * 8192, lds_base(smem) + 32768 + (s >> 1) * 65536 + (s & 1) * 16384 + qb * 4096, l16, l16, l16, l16);
      WAITV(12); BAR();
      WAITV(8); BAR();
      WAITV(4); BAR();
      WAITV(0); BAR();
    }
    WAITV(0);
    return;
  }
  const char* qp = ws + WS_Q + (head * 64 + tb) * 2048 + lane * 16;
  const u32x4 qf[2] = {*(const u32x4*)qp, *(const u32x4*)(qp + 1024)};
  const unsigned* nn = (const unsigned*)(ws + WS_N) + head * 64 + lane;
  const unsigned nq = nn[0], k2 = wave_max_u(nn[1024]), q2 = wave_max_u((lane >> 2) == qt ? nq : 0u);
  const bool safe = __uint_as_float(k2) * __uint_as_float(q2) <= SAFE_T2;
  f32x16 o = {};
  float m = -3.0e38f;
  TS(2)
  if (safe) attn_loop<true>(smem, qf, o, m, kh);
  else attn_loop<false>(smem, qf, o, m, kh);
  TS(3)
  f32x4* red = (f32x4*)(smem + 32768) + qb * 320 + lane;
  if (kh == 1) {
#pragma unroll
    for (int g = 0; g < 4; ++g) red[g * 64] = (f32x4){o[4 * g], o[4 * g + 1], o[4 * g + 2], o[4 * g + 3]};
    ((float*)(red - lane + 256))[lane] = m;
  }
  WAITL();
  BAR();
  if (kh == 0) {
    float a0 = 1.f, a1 = 1.f;
    if (!safe) {
      const float m1 = ((const float*)(red - lane + 256))[lane], mm = fmaxf(m, m1);
      a0 = __builtin_amdgcn_exp2f(m - mm); a1 = __builtin_amdgcn_exp2f(m1 - mm);
    }
#pragma unroll
    for (int g = 0; g < 4; ++g) {
      const f32x4 t = red[g * 64];
#pragma unroll
      for (int i = 0; i < 4; ++i) o[4 * g + i] = o[4 * g + i] * a0 + t[i] * a1;
    }
    const float inv = 1.0f / halves(o[12])[1];
    unsigned u[8];
#pragma unroll
    for (int i = 0; i < 8; ++i) u[i] = pk2(o[2 * i] * inv, o[2 * i + 1] * inv);
    if (lane >= 32) { u[6] = 0u; u[7] = 0u; }
    char* dst = ws + WS_O + (tb * 16 + head) * 2048 + lane * 16;
    const u32x4 o0 = {u[0], u[1], u[2], u[3]}, o1 = {u[4], u[5], u[6], u[7]};
    asm volatile("global_store_dwordx4 %0, %1, off sc1\n\tglobal_store_dwordx4 %0, %2, off offset:1024 sc1\n\ts_waitcnt vmcnt(0)"
                 ::"v"(dst), "v"(o0), "v"(o1) : "memory");
    if (lane == 0) __hip_atomic_fetch_add(cnt + (tb >> 1) * 16, 1u, __ATOMIC_RELAXED, __HIP_MEMORY_SCOPE_AGENT);
  }
  TS(4)
  if (idx < 28) gemm_body<0>(ws, smem, x, nullptr, mt, nt, wv);
  TSEND(1)
}

constexpr int D_U = 16384 + 32, D_STG = 16384 + 2 * D_U, D_NBUF = 3, D_LDS = D_STG * D_NBUF + 1024;

__global__ __launch_bounds__(512) void k_ffn1(char* __restrict__ ws, const u16* __restrict__ wg, const u16* __restrict__ wu,
                                              const u16* __restrict__ wd) {
  extern __shared__ __attribute__((aligned(16))) char smem[];
  TSDECL
  const int wv = __builtin_amdgcn_readfirstlane(threadIdx.x >> 6), wq = wv & 3;
  const int bid = blockIdx.x, xcd = bid & 7, idx = bid >> 3;
  const int mt = (xcd >> 1) * 4 + idx / 7, nt = (xcd & 1) * 7 + idx % 7;
  const int lane = threadIdx.x & 63, r = lane & 31, h = lane >> 5;
  const int tb = mt * 4 + wq;
  float* cs = (float*)(smem + D_STG * D_NBUF);
  if (wv >= 4) {
    int src[4];
#pragma unroll
    for (int i = 0; i < 4; ++i)
      src[i] = ((wq & 1) * 32 + 4 * i + (lane >> 4)) * (F_ * 2) + nt * 256 + (((lane & 15) ^ ((lane >> 4) << 2)) << 4) - 1024 * i;
    const char* wsrc = (const char*)(wq < 2 ? wg : wu);
    const char* hsrc = ws + WS_H + tb * (28 * 1024);
    const int l16 = lane * 16, t = wq * 64 + lane;
    float cp[28];
    ring_loader<7, D_NBUF, D_STG, 12>([&](int s, int so) {
      const unsigned buf = lds_base(smem) + so + 16384 + (wq >> 1) * D_U + (wq & 1) * 8192;
      dma4(hsrc + s * 4096, lds_base(smem) + so + wq * 4096, l16, l16, l16, l16);
      dma4(wsrc + s * (64 * F_ * 2), buf, src[0], src[1], src[2], src[3]);
      dma4(wsrc + (s * 64 + 16) * (F_ * 2), buf + 4096, src[0], src[1], src[2], src[3]);
    }, [&](int s) {
      if (s == 3) {
        const float* p = (const float*)(ws + WS_CS) + ((t >> 4) & 1) * F_ + nt * 128 + (t >> 5) * 16 + (t & 15);
#pragma unroll
        for (int i = 0; i < 28; ++i) cp[i] = p[i * 2 * F_];
      }
      if (s == 5) {
        const unsigned junk = lds_base(smem) + (5 % D_NBUF) * D_STG + wq * 256;
        touch(wd, (idx * 448 + t) * 128, junk);
        touch(wd, (idx * 448 + 192 + t) * 128, junk);
        WAITV(0);
      }
      if (s == 4) {
        float c = 0.f;
#pragma unroll
        for (int i = 0; i < 28; ++i) c = fma1(cp[i], 1.0f, c);
        cs[t] = c;
      }
    });
    return;
  }
  const f32x2* sp = (const f32x2*)(ws + WS_ST) + (tb * 14 + 7 * h) * 32 + r;
  f32x2 ps[7];
#pragma unroll
  for (int i = 0; i < 7; ++i) ps[i] = sp[i * 32];
  const int q4 = (lane & 15) >> 2, p4 = lane & 3, blk = (lane >> 4) & 1;
  int bo[4];
#pragma unroll
  for (int i = 0; i < 4; ++i) bo[i] = 16384 + blk * D_U + (4 * h + q4) * 256 + ((i ^ q4) << 6) + 8 * p4;
  const char* ap = smem + wq * 4096 + lane * 16;
  f32x16 acc[8];
#pragma unroll
  for (int b = 0; b < 8; ++b)
#pragma unroll
    for (int i = 0; i < 16; ++i) acc[b][i] = 0.f;
  const f32x2 tot = ((ps[0] + ps[1]) + (ps[2] + ps[3])) + ((ps[4] + ps[5]) + ps[6]);
  f32x2 st = ln_scalars(sum32(tot[0]), sum32(tot[1]));
  PIN1(st);
  u32x4 w[2][8], a[2];
  SB();
  BAR();
  TS(2)
  a[0] = lds_r128(ap);
#pragma unroll
  for (int b = 0; b < 8; ++b) w[0][b] = lds_tr2(smem + bo[b >> 1] + (b & 1) * 32, smem + bo[b >> 1] + (b & 1) * 32 + 2048);
  SB();
  int so = 0;
#pragma unroll 1
  for (int s = 0; s < 7; ++s) {
#pragma unroll
    for (int j = 0; j < 4; ++j) {
      const int c = j & 1, n = c ^ 1, jn = (j + 1) & 3;
      __builtin_amdgcn_s_waitcnt(0xc07f);
      if (j == 3) {
        acc[0] = mfma_u(w[c][0], a[c], acc[0]);
        SB();
        if (s < 6) BAR();
        so = so == (D_NBUF - 1) * D_STG ? 0 : so + D_STG;
        SB();
      }
      const char* p = smem + so + jn * 4096;
      a[n] = lds_r128(ap + so + jn * 1024);
#pragma unroll
      for (int b = 0; b < 8; ++b) w[n][b] = lds_tr2(p + bo[b >> 1] + (b & 1) * 32, p + bo[b >> 1] + (b & 1) * 32 + 2048);
#pragma unroll
      for (int b = j == 3 ? 1 : 0; b < 8; ++b) acc[b] = mfma_u(w[c][b], a[c], acc[b]);
      if (j == 3) SG(0x100, 4);
#pragma unroll
      for (int b = 0; b < 5; ++b) { SG(0x008, 1); SG(0x100, 4); }
      SG(0x008, 3);
      SB();
    }
  }
  TS(9)
  const float k1 = st[1] * (-2.0f * 0.7978845608028654f * 1.4426950408889634f);
  const float k3 = k1 * st[1] * st[1] * 0.044715f, i2 = 1.0f / (st[1] * st[1]);
  const f32x2 c1 = {k1, k1}, c3 = {k3, k3}, ir2 = {i2, i2}, nm = {-st[0], -st[0]};
  char* dst = ws + WS_ACT + (size_t)(tb * 112 + nt * 8) * 1024 + lane * 16;
#pragma unroll
  for (int b = 0; b < 8; ++b) {
    u32x4 o;
    f32x4 c4[4];
#pragma unroll
    for (int g = 0; g < 4; ++g) c4[g] = *(const f32x4*)(cs + b * 32 + 8 * g + 4 * h);
#pragma unroll
    for (int i = 0; i < 4; ++i) {
      const int q = i >> 1, e = (i & 1) * 2;
      const f32x2 g = __builtin_elementwise_fma(nm, (f32x2){c4[q][e], c4[q][e + 1]}, (f32x2){acc[b][2 * i], acc[b][2 * i + 1]});
      const f32x2 u = __builtin_elementwise_fma(nm, (f32x2){c4[2 + q][e], c4[2 + q][e + 1]}, (f32x2){acc[b][8 + 2 * i], acc[b][9 + 2 * i]});
      const f32x2 z = __builtin_elementwise_fma(g * g, c3, c1) * g;
      const f32x2 d = __builtin_elementwise_fma((f32x2){__builtin_amdgcn_exp2f(z[0]), __builtin_amdgcn_exp2f(z[1])}, ir2, ir2);
      const f32x2 v = (g * u) * (f32x2){__builtin_amdgcn_rcpf(d[0]), __builtin_amdgcn_rcpf(d[1])};
      o[i] = pk2(v[0], v[1]);
    }
    *(u32x4*)(dst + b * 1024) = o;
  }
  TSEND(3)
}

constexpr int C_LDS = 4 * 32768;
__global__ __launch_bounds__(768) void k_down(char* __restrict__ ws, const u16* __restrict__ B, u16* __restrict__ out) {
  extern __shared__ __attribute__((aligned(16))) char smem[];
  TSDECL
  const int lane = threadIdx.x & 63, wv = __builtin_amdgcn_readfirstlane(threadIdx.x >> 6);
  const int bid = blockIdx.x, xcd = bid & 7, idx = bid >> 3;
  const int mt = xcd * 4 + idx / 7, nt = idx % 7;
  if (wv >= 8) {
    const int lw = wv - 8, l16 = lane * 16;
    const char* A = ws + WS_ACT + (size_t)(mt * 2 + (lw >> 1)) * 112 * 1024 + (lw & 1) * 4096;
    int bs[4];
    gemm_bsrc<1>(bs, lw, lane, nt);
    ring_loader<14, 4, 32768, 8>([&](int s, int so) {
      const unsigned buf = lds_base(smem) + so + lw * 4096;
      dma4(B + s * 128 * D_, buf + 16384, bs[0], bs[1], bs[2], bs[3]);
      dma4(A + s * 8192, buf, l16, l16, l16, l16);
    });
    return;
  }
  gemm_body<1>(ws, smem, nullptr, out, mt, nt, wv);
  TSEND(4)
}

__device__ __noinline__ void mark_hi() { asm volatile("s_nop 1"); }

extern "C" void kernel_launch(void* const* d_in, const int* in_sizes, int n_in, void* d_out, int out_size, void* d_ws,
                              size_t ws_size, hipStream_t stream) {
  const u16* x = (const u16*)d_in[0];
  const u16* wqkv = (const u16*)d_in[1];
  const u16* wo = (const u16*)d_in[2];
  const u16* wg = (const u16*)d_in[4];
  const u16* wu = (const u16*)d_in[5];
  const u16* wd = (const u16*)d_in[6];
  u16* out = (u16*)d_out;
  char* ws = (char*)d_ws;
  (void)hipFuncSetAttribute((const void*)k_qkv, hipFuncAttributeMaxDynamicSharedMemorySize, A_LDS);
  (void)hipFuncSetAttribute((const void*)k_attn, hipFuncAttributeMaxDynamicSharedMemorySize, B_LDS);
  (void)hipFuncSetAttribute((const void*)k_down, hipFuncAttributeMaxDynamicSharedMemorySize, C_LDS);
  (void)hipFuncSetAttribute((const void*)k_ffn1, hipFuncAttributeMaxDynamicSharedMemorySize, D_LDS);
  k_qkv<<<256, 768, A_LDS, stream>>>(x, wqkv, ws);
  k_attn<<<256, 768, B_LDS, stream>>>(ws, wo, wg, wu, x);
  k_ffn1<<<224, 512, D_LDS, stream>>>(ws, wg, wu, wd);
  k_down<<<224, 768, C_LDS, stream>>>(ws, wd, out);
}
```

```cpp
#include <hip/hip_runtime.h>
#include <hip/hip_bf16.h>

#define WTMOD "sc1"
typedef unsigned short u16;
typedef __attribute__((ext_vector_type(8))) short bf16x8;
typedef __attribute__((ext_vector_type(4))) short s16x4;
typedef __attribute__((ext_vector_type(2))) __bf16 b16x2;
typedef __attribute__((ext_vector_type(2))) float f32x2;
typedef __attribute__((ext_vector_type(4))) float f32x4;
typedef __attribute__((ext_vector_type(16))) float f32x16;
typedef __attribute__((ext_vector_type(4))) unsigned u32x4;
typedef __attribute__((ext_vector_type(2))) unsigned u32x2;
#define LAS __attribute__((address_space(3)))
#define DI __device__ __forceinline__
#define WAITV(n) asm volatile("s_waitcnt vmcnt(%0) lgkmcnt(0)" ::"i"(n) : "memory")
#define WAITL() asm volatile("s_waitcnt lgkmcnt(0)" ::: "memory")
#define BAR() __builtin_amdgcn_s_barrier()
#define TSDECL
#define TS(i)
#define TSEND(kid)

__device__ __noinline__ void mark_lo() { asm volatile("s_nop 0"); }
__device__ __noinline__ void mark_hi();
constexpr int D_ = 448, DH_ = 28, F_ = 1792, N3_ = 1344;
constexpr float LN_EPS = 1e-5f;
constexpr float QSCALE = 0.18898223650461362f * 1.4426950408889634f;
constexpr float SAFE_T2 = 3600.0f;
constexpr unsigned WS_Q = 0, WS_KV = 2u << 20, WS_O = 6u << 20, WS_H = 8u << 20, WS_ACT = 10u << 20, WS_N = 18u << 20, WS_ST = 19u << 20, WS_CS = 20u << 20, WS_CNT = 21u << 20;

DI unsigned pk2(float lo, float hi) {
  f32x2 f = {lo, hi};
  return __builtin_bit_cast(unsigned, __builtin_convertvector(f, b16x2));
}
DI float bf_lo(unsigned u) { return __uint_as_float(u << 16); }
DI float bf_hi(unsigned u) { return __uint_as_float(u & 0xffff0000u); }
DI float rbf(float v) { return bf_lo(pk2(v, v)); }
DI float dot2(unsigned a, unsigned b, float c) {
  return __builtin_amdgcn_fdot2_f32_bf16(__builtin_bit_cast(b16x2, a), __builtin_bit_cast(b16x2, b), c, false);
}
DI float fma1(float a, float b, float c) {
  float d;
  asm("v_fma_f32 %0, %1, %2, %3" : "=v"(d) : "v"(a), "v"(b), "v"(c));
  return d;
}
constexpr unsigned ONES2 = 0x3f803f80u;
#define DMA_HEAD "s_mov_b32 %0, m0\n\ts_mov_b32 m0, %1\n\ts_nop 0\n\t"
#define DMA_L(v, o) "global_load_lds_dwordx4 %" #v ", %2 offset:" #o "\n\t"
#define DMA_TAIL "s_mov_b32 m0, %0"
#define DMA_C(v, o) "global_load_lds_dwordx4 %" #v ", %2 offset:" #o " sc1\n\t"
DI void dma4c(const void* sb, unsigned lds, int a) {
  unsigned k;
  asm volatile(DMA_HEAD DMA_C(3, 0) DMA_C(3, 1024) DMA_C(3, 2048) DMA_C(3, 3072) DMA_TAIL
               : "=&s"(k) : "s"(lds), "s"(sb), "v"(a) : "memory");
}
DI void dma2(const void* sb, unsigned lds, int a, int b) {
  unsigned k;
  asm volatile(DMA_HEAD DMA_L(3, 0) DMA_L(4, 1024) DMA_TAIL : "=&s"(k) : "s"(lds), "s"(sb), "v"(a), "v"(b) : "memory");
}
DI void dma3(const void* sb, unsigned lds, int a, int b, int c) {
  unsigned k;
  asm volatile(DMA_HEAD DMA_L(3, 0) DMA_L(4, 1024) DMA_L(5, 2048) DMA_TAIL
               : "=&s"(k) : "s"(lds), "s"(sb), "v"(a), "v"(b), "v"(c) : "memory");
}
DI void dma4(const void* sb, unsigned lds, int a, int b, int c, int d) {
  unsigned k;
  asm volatile(DMA_HEAD DMA_L(3, 0) DMA_L(4, 1024) DMA_L(5, 2048) DMA_L(6, 3072) DMA_TAIL
               : "=&s"(k) : "s"(lds), "s"(sb), "v"(a), "v"(b), "v"(c), "v"(d) : "memory");
}
DI void touch(const void* sb, int voff, unsigned lds) {
  unsigned k;
  asm volatile(DMA_HEAD "global_load_lds_dword %3, %2\n\t" DMA_TAIL : "=&s"(k) : "s"(lds), "s"(sb), "v"(voff) : "memory");
}
#define SB() __builtin_amdgcn_sched_barrier(0)
#define SG(m, n) __builtin_amdgcn_sched_group_barrier(m, n, 0)
#define PIN2(a, b) asm volatile("" : "+v"(a), "+v"(b))
#define PIN1(a) asm volatile("" : "+v"(a))
DI unsigned lds_base(char* smem) { return (unsigned)(size_t)(LAS char*)smem; }
DI u32x4 lds_r128(const char* p) { return *(const u32x4*)p; }
DI u32x4 lds_tr2(const char* p0, const char* p1) {
  s16x4 a = __builtin_amdgcn_ds_read_tr16_b64_v4i16((LAS s16x4*)p0);
  s16x4 b = __builtin_amdgcn_ds_read_tr16_b64_v4i16((LAS s16x4*)p1);
  u32x2 ua = __builtin_bit_cast(u32x2, a), ub = __builtin_bit_cast(u32x2, b);
  u32x4 r = {ua[0], ua[1], ub[0], ub[1]};
  return r;
}
DI f32x16 mfma_u(u32x4 a, u32x4 b, f32x16 c) {
  return __builtin_amdgcn_mfma_f32_32x32x16_bf16(__builtin_bit_cast(bf16x8, a), __builtin_bit_cast(bf16x8, b), c, 0, 0, 0);
}
DI f32x2 halves(float v) {
  auto r = __builtin_amdgcn_permlane32_swap(__float_as_uint(v), __float_as_uint(v), false, false);
  return (f32x2){__uint_as_float(r[0]), __uint_as_float(r[1])};
}
DI float sum32(float v) { f32x2 t = halves(v); return t[0] + t[1]; }
DI unsigned wave_max_u(unsigned u) {
  u = max(u, (unsigned)__builtin_amdgcn_update_dpp(0, (int)u, 0x128, 0xf, 0xf, false));
  u = max(u, (unsigned)__builtin_amdgcn_update_dpp(0, (int)u, 0x124, 0xf, 0xf, false));
  u = max(u, (unsigned)__builtin_amdgcn_update_dpp(0, (int)u, 0x122, 0xf, 0xf, false));
  u = max(u, (unsigned)__builtin_amdgcn_update_dpp(0, (int)u, 0x121, 0xf, 0xf, false));
  unsigned a = __builtin_amdgcn_readlane(u, 0), b = __builtin_amdgcn_readlane(u, 16);
  unsigned c = __builtin_amdgcn_readlane(u, 32), d = __builtin_amdgcn_readlane(u, 48);
  return max(max(a, b), max(c, d));
}
DI void st_wt(void* p, u32x4 v) {
  asm volatile("global_store_dwordx4 %0, %1, off " WTMOD "\n\ts_nop 1" ::"v"(p), "v"(v) : "memory");
}
DI void spin_until(unsigned* c, unsigned n) {
  while (__hip_atomic_load(c, __ATOMIC_RELAXED, __HIP_MEMORY_SCOPE_AGENT) < n) __builtin_amdgcn_s_sleep(2);
}
DI f32x2 ln_scalars(float sum, float sq) {
  const float mean = sum * (1.0f / D_);
  float var = sq * (1.0f / D_) - mean * mean;
  var = var > 0.f ? var : 0.f;
  const float sd = rbf(sqrtf(rbf(rbf(var) + LN_EPS)));
  return (f32x2){rbf(mean), 1.0f / sd};
}

template <int ND, int E4, int E7>
constexpr int ring_allow(int k, int last) {
  int n = (last - k) * ND;
  if (k <= 4 && last >= 4) n += E4;
  if (k <= 7 && last >= 7) n += E7;
  return n;
}
#define RP 2
#define RR 2
template <int NS, int NBUF>
constexpr int ring_cnt(int s) {
  int n = RP + (NBUF == 5 ? 1 : RR) * (s + 1);
  n = n < s + NBUF + (s < 0) ? n : s + NBUF + (s < 0);
  return n < NS ? n : NS;
}
struct NoHook { DI void operator()(int) const {} };
template <int NS, int NBUF, int STG, int ND, int E4 = 0, int E7 = 0, class F, class H = NoHook>
DI void ring_loader(F issue, H idle = H()) {
#pragma unroll
  for (int s = -1; s < NS - 1; ++s) {
#pragma unroll
    for (int k = s < 0 ? 0 : ring_cnt<NS, NBUF>(s - 1); k < ring_cnt<NS, NBUF>(s); ++k) issue(k, (k % NBUF) * STG);
    if (s > 0) idle(s - 1);
    WAITV((ring_allow<ND, E4, E7>(s + 1, ring_cnt<NS, NBUF>(s) - 1)));
    BAR();
  }
  idle(NS - 2);
}

constexpr int A_XB = 128 * 128, A_WB = 64 * 192, A_STG = A_XB + A_WB, A_NBUF = 5;
constexpr int A_MISC = A_STG * A_NBUF;
constexpr int A_LDS = A_MISC + 5 * 96 * 4 + 3 * 128 * 8 + 256;
#define TSLEEP 20

template <int GRP, bool ISV>
DI void qkv_body(char* __restrict__ ws, char* smem, int mt, int nt, int wq) {
  TSDECL
  constexpr int NB = GRP == 0 ? 2 : 1, B0 = GRP == 0 ? 0 : 2;
  const int lane = threadIdx.x & 63, r = lane & 31, h = lane >> 5;
  const int off = (84 * nt) & 7;
  f32x16 acc[NB];
  float cs[NB];
#pragma unroll
  for (int b = 0; b < NB; ++b) {
    cs[b] = 0.f;
#pragma unroll
    for (int i = 0; i < 16; ++i) acc[b][i] = 0.f;
  }
  float sum = 0.f, sq = 0.f, sum1 = 0.f, sq1 = 0.f;
  const int xrow = 32 * wq + r, xsw = (xrow >> 1) & 7;
  const int q4 = (lane & 15) >> 2, p4 = lane & 3, blk = (lane >> 4) & 1;
  int ao[4], bo[4];
#pragma unroll
  for (int p = 0; p < 4; ++p) {
    const int kk = (p + wq) & 3;
    ao[p] = xrow * 128 + (((2 * kk + h) ^ xsw) << 4);
    bo[p] = A_XB + (16 * kk + 8 * h + q4) * 192 + (off + 16 * blk + 4 * p4) * 2 + B0 * 56;
  }
  struct Fr { u32x4 a[4], w[4][NB]; } f0, f1;
  auto rd = [&](Fr& f, int so) {
#pragma unroll
    for (int p = 0; p < 4; ++p) {
      f.a[p] = lds_r128(smem + so + ao[p]);
#pragma unroll
      for (int b = 0; b < NB; ++b) f.w[p][b] = lds_tr2(smem + so + bo[p] + b * 56, smem + so + bo[p] + b * 56 + 4 * 192);
    }
  };
  auto mf = [&](const Fr& f) {
#pragma unroll
    for (int p = 0; p < 4; ++p)
#pragma unroll
      for (int b = 0; b < NB; ++b) acc[b] = ISV ? mfma_u(f.a[p], f.w[p][b], acc[b]) : mfma_u(f.w[p][b], f.a[p], acc[b]);
#pragma unroll
    for (int i = 0; i < 4 * NB; ++i) { SG(0x008, 1); SG(0x100, 3); }
    SB();
  };
  auto dt = [&](const Fr& f) {
#pragma unroll
    for (int p = GRP; p < (GRP ? 4 : 1); ++p) {
#pragma unroll
      for (int i = 0; i < 4; i += 2) {
        sum = dot2(f.a[p][i], ONES2, sum); sq = dot2(f.a[p][i], f.a[p][i], sq);
        sum1 = dot2(f.a[p][i + 1], ONES2, sum1); sq1 = dot2(f.a[p][i + 1], f.a[p][i + 1], sq1);
      }
    }
    PIN2(sum, sq); PIN2(sum1, sq1);
#pragma unroll
    for (int i = 0; i < 4; ++i)
#pragma unroll
      for (int b = 0; b < NB; ++b) cs[b] = dot2(f.w[0][b][i], ONES2, cs[b]);
#pragma unroll
    for (int b = 0; b < NB; ++b) PIN1(cs[b]);
    SB();
  };
  BAR();
  TS(2)
  rd(f0, 0);
  SB();
  int so = 0;
#pragma unroll 1
  for (int s = 0; s < 6; s += 2) {
    __builtin_amdgcn_s_waitcnt(0xc07f);
    BAR();
    SB();
    so = so == (A_NBUF - 1) * A_STG ? 0 : so + A_STG;
    rd(f1, so); mf(f0); dt(f0);
    __builtin_amdgcn_s_waitcnt(0xc07f);
    BAR();
    SB();
    so = so == (A_NBUF - 1) * A_STG ? 0 : so + A_STG;
    rd(f0, so); mf(f1); dt(f1);
  }
  dt(f0);
  TS(9)
  float* csp = (float*)(smem + A_MISC);
  f32x2* tst = (f32x2*)(smem + A_MISC + 5 * 96 * 4);
#pragma unroll
  for (int b = 0; b < NB; ++b) {
    const float c = sum32(cs[b]);
    if (h == 0) csp[wq * 96 + (B0 + b) * 32 + r] = c;
  }
  {
    const f32x2 pr = {sum32(sum + sum1), sum32(sq + sq1)};
    if (h == 0) tst[128 + GRP * 128 + 32 * wq + r] = pr;
  }
  WAITL();
  BAR();
  SB();
  mf(f0);
  BAR();
  const float* ct = csp + 384;
  const int tb = mt * 4 + wq;
#pragma unroll
  for (int b = 0; b < NB; ++b) {
    const int gb = 3 * nt + B0 + b, head = gb & 15, type = gb >> 4, cb = (B0 + b) * 32;
    unsigned o[8];
    if (ISV) {
      const float c = ct[cb + r];
#pragma unroll
      for (int g = 0; g < 4; ++g) {
        const float* sp = (const float*)(tst + 32 * wq + 8 * g + 4 * h);
        f32x4 s0 = *(const f32x4*)sp, s1 = *(const f32x4*)(sp + 4);
        float v0 = s0[1] * (acc[b][4 * g] - s0[0] * c), v1 = s0[3] * (acc[b][4 * g + 1] - s0[2] * c);
        float v2 = s1[1] * (acc[b][4 * g + 2] - s1[0] * c), v3 = s1[3] * (acc[b][4 * g + 3] - s1[2] * c);
        o[2 * g] = pk2(v0, v1); o[2 * g + 1] = pk2(v2, v3);
      }
      if (r >= DH_) {
        const unsigned f = (r == DH_) ? ONES2 : 0u;
#pragma unroll
        for (int i = 0; i < 8; ++i) o[i] = f;
      }
      char* dst = ws + WS_KV + (head * 64 + tb) * 4096 + 2048 + lane * 16;
      st_wt(dst, (u32x4){o[0], o[1], o[2], o[3]});
      st_wt(dst + 1024, (u32x4){o[4], o[5], o[6], o[7]});
    } else {
      const f32x2 st = tst[32 * wq + r];
      const float sc = type == 0 ? st[1] * QSCALE : st[1];
      float n2 = 0.f;
#pragma unroll
      for (int g = 0; g < 4; ++g) {
        const f32x4 c4 = *(const f32x4*)(ct + cb + 8 * g + 4 * h);
        float v0 = sc * (acc[b][4 * g] - st[0] * c4[0]), v1 = sc * (acc[b][4 * g + 1] - st[0] * c4[1]);
        float v2 = sc * (acc[b][4 * g + 2] - st[0] * c4[2]), v3 = sc * (acc[b][4 * g + 3] - st[0] * c4[3]);
        unsigned a = pk2(v0, v1), bb = pk2(v2, v3);
        if (g == 3 && h == 1) { a = 0u; bb = 0u; }
        o[2 * g] = a; o[2 * g + 1] = bb;
        n2 = dot2(a, a, n2); n2 = dot2(bb, bb, n2);
      }
      char* dst = ws + (type == 0 ? WS_Q + (head * 64 + tb) * 2048 : WS_KV + (head * 64 + tb) * 4096) + lane * 16;
      st_wt(dst, (u32x4){o[0], o[1], o[2], o[3]});
      st_wt(dst + 1024, (u32x4){o[4], o[5], o[6], o[7]});
      const unsigned mx = wave_max_u(__float_as_uint(sum32(n2)));
      if (lane == 0) __hip_atomic_store((unsigned*)(ws + WS_N) + type * 1024 + head * 64 + tb, mx, __ATOMIC_RELAXED, __HIP_MEMORY_SCOPE_AGENT);
    }
  }
  TSEND(0)
}

template <int MODE>
DI void gemm_bsrc(int (&bs)[4], int lw, int lane, int nt) {
#pragma unroll
  for (int i = 0; i < 4; ++i) {
    int row = 32 * lw + 8 * i + (lane >> 3);
    const int cb = (lane & 7) ^ (((row >> 1) & 1) << 2);
    if (MODE == 0 && row >= 112) row -= 16;
    bs[i] = (row * D_ + nt * 64 + 8 * cb) * 2 - 1024 * i;
  }
}
template <int MODE>
DI void gemm_body(char* __restrict__ ws, char* smem, const u16* __restrict__ x, u16* __restrict__ out, int mt, int nt, int wv) {
  constexpr int NS = MODE ? 14 : 4, NBUF = 4, STG = 32768;
  const int lane = threadIdx.x & 63, r = lane & 31, h = lane >> 5;
  const int tbl = wv & 1, cbk = (wv >> 1) & 1, kh = wv >> 2;
  u32x4 rv[2];
  char* hp = ws + WS_H + ((mt * 2 + tbl) * 28 + nt * 4 + cbk * 2) * 1024 + lane * 16;
  if (kh == 0) {
    if (MODE) { rv[0] = *(const u32x4*)hp; rv[1] = *(const u32x4*)(hp + 1024); }
    else {
      const u16* rp = x + (size_t)(mt * 64 + tbl * 32 + r) * D_ + nt * 64 + cbk * 32 + 4 * h;
#pragma unroll
      for (int g = 0; g < 4; ++g) { u32x2 t = *(const u32x2*)(rp + 8 * g); rv[g >> 1][2 * (g & 1)] = t[0]; rv[g >> 1][2 * (g & 1) + 1] = t[1]; }
    }
  }
  f32x16 acc = {};
  const int q4 = (lane & 15) >> 2, p4 = lane & 3, blk = (lane >> 4) & 1;
  const int bcol = 32 * cbk + 16 * blk + 4 * p4;
  const int aoff = (MODE ? 0 : 32768) + tbl * 8192 + kh * 4096 + lane * 16;
  const int boff = (MODE ? 16384 : 0) + ((MODE ? 64 : 56) * kh + 4 * h + q4) * 128 + (((bcol >> 3) ^ ((q4 >> 1) << 2)) << 4) + (bcol & 7) * 2;
  struct Fr { u32x4 a[4], w[4]; } f0, f1;
  auto rd = [&](Fr& f, int so) {
#pragma unroll
    for (int ks = 0; ks < 4; ++ks) {
      f.a[ks] = lds_r128(smem + so + aoff + ks * 1024);
      const char* p = smem + so + boff + (MODE ? 16 * ks : 28 * (ks >> 1) + 16 * (ks & 1)) * 128;
      f.w[ks] = lds_tr2(p, p + 8 * 128);
    }
    SB();
  };
  auto mm = [&](const Fr& f) {
#pragma unroll
    for (int ks = 0; ks < 4; ++ks) acc = mfma_u(f.w[ks], f.a[ks], acc);
    SB();
  };
  if (MODE == 0) BAR();
  BAR();
  rd(f0, 0);
#pragma unroll 1
  for (int s = 0; s < NS; s += 2) {
    WAITL();
    BAR();
    rd(f1, MODE ? ((s + 1) & 3) * STG : s * 32768 + 16384); mm(f0);
    if (s + 2 < NS) { WAITL(); BAR(); rd(f0, MODE ? ((s + 2) & 3) * STG : 65536); }
    mm(f1);
  }
  char* fr = smem + (NS % NBUF) * STG;
  f32x4* red = (f32x4*)fr + (wv & 3) * 256 + lane;
  if (kh == 1) {
#pragma unroll
    for (int g = 0; g < 4; ++g) red[g * 64] = (f32x4){acc[4 * g], acc[4 * g + 1], acc[4 * g + 2], acc[4 * g + 3]};
  }
  WAITL();
  BAR();
  if (kh == 0) {
    unsigned u[8];
#pragma unroll
    for (int g = 0; g < 4; ++g) {
      const f32x4 t = red[g * 64];
      const unsigned t0 = pk2(acc[4 * g] + t[0], acc[4 * g + 1] + t[1]), t1 = pk2(acc[4 * g + 2] + t[2], acc[4 * g + 3] + t[3]);
      const unsigned r0 = rv[g >> 1][2 * (g & 1)], r1 = rv[g >> 1][2 * (g & 1) + 1];
      u[2 * g] = pk2(bf_lo(t0) + bf_lo(r0), bf_hi(t0) + bf_hi(r0));
      u[2 * g + 1] = pk2(bf_lo(t1) + bf_lo(r1), bf_hi(t1) + bf_hi(r1));
    }
    if (MODE == 0) {
      st_wt(hp, (u32x4){u[0], u[1], u[2], u[3]});
      st_wt(hp + 1024, (u32x4){u[4], u[5], u[6], u[7]});
      float s0 = 0.f, s1 = 0.f, q0 = 0.f, q1 = 0.f;
#pragma unroll
      for (int i = 0; i < 8; i += 2) {
        s0 = dot2(u[i], ONES2, s0); q0 = dot2(u[i], u[i], q0);
        s1 = dot2(u[i + 1], ONES2, s1); q1 = dot2(u[i + 1], u[i + 1], q1);
      }
      const f32x2 st = {sum32(s0 + s1), sum32(q0 + q1)};
      if (h == 0)
        __hip_atomic_store((unsigned long long*)(ws + WS_ST) + ((mt * 2 + tbl) * 14 + nt * 2 + cbk) * 32 + r,
                           __builtin_bit_cast(unsigned long long, st), __ATOMIC_RELAXED, __HIP_MEMORY_SCOPE_AGENT);
    } else {
      char* tp = fr + 16384 + (tbl * 32 + r) * 136 + cbk * 64 + h * 8;
#pragma unroll
      for (int g = 0; g < 4; ++g) *(u32x2*)(tp + 16 * g) = (u32x2){u[2 * g], u[2 * g + 1]};
    }
  }
  if (MODE == 1) {
    WAITL();
    BAR();
    const int row = wv * 8 + (lane >> 3);
    const char* tp = fr + 16384 + row * 136 + (lane & 7) * 16;
    const u32x2 a = *(const u32x2*)tp, b = *(const u32x2*)(tp + 8);
    st_wt(out + (size_t)(mt * 64 + row) * D_ + nt * 64 + (lane & 7) * 8, (u32x4){a[0], a[1], b[0], b[1]});
  }
}

constexpr int B_LDS = 4 * 32768 + 1024;

template <bool SAFE>
DI void attn_loop(char* smem, const u32x4 (&qf)[2], f32x16& o, float& m, int kh) {
  const int lane = threadIdx.x & 63;
  const f32x16 zero = {};
  BAR();
  const char* lp = smem + kh * 16384 + lane * 16;
  u32x4 kk[2][2], vv[4][2];
  f32x16 sc[2];
  kk[0][0] = lds_r128(lp); kk[0][1] = lds_r128(lp + 1024);
  kk[1][0] = lds_r128(lp + 4096); kk[1][1] = lds_r128(lp + 5120);
  vv[0][0] = lds_r128(lp + 2048); vv[0][1] = lds_r128(lp + 3072);
  vv[1][0] = lds_r128(lp + 6144); vv[1][1] = lds_r128(lp + 7168);
  sc[0] = mfma_u(kk[0][1], qf[1], mfma_u(kk[0][0], qf[0], zero));
  SB();
  if (SAFE) {
    u32x4 p0, p1 = {0u, 0u, 0u, 0u};
    vv[3][1] = p1;
#define EX2(d, i) d = pk2(__builtin_amdgcn_exp2f(cur[i]), __builtin_amdgcn_exp2f(cur[(i) + 1]))
#pragma unroll 1
    for (int s = 0; s < 8; ++s) {
#pragma unroll
      for (int b = 0; b < 4; ++b) {
        f32x16& cur = sc[b & 1];
        f32x16& nxt = sc[(b + 1) & 1];
        const char* np = lp + (b >= 2 ? ((s + 1) & 3) * 32768 + (b - 2) * 4096 : (s & 3) * 32768 + (b + 2) * 4096);
        nxt = mfma_u(kk[(b + 1) & 1][0], qf[0], zero);
        SB();
        if (b == 2 && s < 7) BAR();
        kk[b & 1][0] = lds_r128(np);
        EX2(p0[0], 0); EX2(p0[1], 2);
        SB();
        o = mfma_u(vv[(b + 3) & 3][1], p1, o);
        SB();
        kk[b & 1][1] = lds_r128(np + 1024);
        EX2(p0[2], 4); EX2(p0[3], 6);
        SB();
        nxt = mfma_u(kk[(b + 1) & 1][1], qf[1], nxt);
        SB();
        vv[(b + 2) & 3][0] = lds_r128(np + 2048);
        EX2(p1[0], 8); EX2(p1[1], 10);
        SB();
        o = mfma_u(vv[b][0], p0, o);
        SB();
        vv[(b + 2) & 3][1] = lds_r128(np + 3072);
        EX2(p1[2], 12); EX2(p1[3], 14);
        if (b == 1) WAITL();
        SB();
      }
    }
    o = mfma_u(vv[3][1], p1, o);
    return;
  }
#pragma unroll 1
  for (int s = 0; s < 8; ++s) {
#pragma unroll
    for (int b = 0; b < 4; ++b) {
      f32x16& cur = sc[b & 1];
      f32x16& nxt = sc[(b + 1) & 1];
      nxt = mfma_u(kk[(b + 1) & 1][0], qf[0], zero);
      SB();
      if (b == 2 && s < 7) {
        WAITL();
        BAR();
      }
      const char* np = lp + (b >= 2 ? ((s + 1) & 3) * 32768 + (b - 2) * 4096 : (s & 3) * 32768 + (b + 2) * 4096);
      kk[b & 1][0] = lds_r128(np); kk[b & 1][1] = lds_r128(np + 1024);
      vv[(b + 2) & 3][0] = lds_r128(np + 2048); vv[(b + 2) & 3][1] = lds_r128(np + 3072);
      SB();
      if (!SAFE) {
        float tm = cur[0];
#pragma unroll
        for (int i = 1; i < 16; ++i) tm = fmaxf(tm, cur[i]);
        const f32x2 t2 = halves(tm);
        const float mn = fmaxf(m, fmaxf(t2[0], t2[1]));
        const float al = __builtin_amdgcn_exp2f(m - mn);
        m = mn;
#pragma unroll
        for (int i = 0; i < 16; ++i) { o[i] *= al; cur[i] -= mn; }
      }
      u32x4 p0, p1;
#pragma unroll
      for (int i = 0; i < 4; ++i) p0[i] = pk2(__builtin_amdgcn_exp2f(cur[2 * i]), __builtin_amdgcn_exp2f(cur[2 * i + 1]));
      SB();
      nxt = mfma_u(kk[(b + 1) & 1][1], qf[1], nxt);
      o = mfma_u(vv[b][0], p0, o);
      SB();
#pragma unroll
      for (int i = 0; i < 4; ++i)
        p1[i] = pk2(__builtin_amdgcn_exp2f(cur[8 + 2 * i]), __builtin_amdgcn_exp2f(cur[8 + 2 * i + 1]));
      SB();
      o = mfma_u(vv[b][1], p1, o);
      SB();
    }
  }
}

__global__ __launch_bounds__(768) void k_attn(char* __restrict__ ws, const u16* __restrict__ wo, const u16* __restrict__ wg,
                                              const u16* __restrict__ wu, const u16* __restrict__ x) {
  extern __shared__ __attribute__((aligned(16))) char smem[];
  TSDECL
  const int lane = threadIdx.x & 63, wv = __builtin_amdgcn_readfirstlane(threadIdx.x >> 6);
  const int bid = blockIdx.x, xcd = bid & 7, idx = bid >> 3;
  const int mt = xcd * 4 + idx / 7, nt = idx % 7;
  unsigned* cnt = (unsigned*)(ws + WS_CNT);
  const int head = xcd * 2 + (idx >> 4), qt = idx & 15, qb = wv & 3, kh = wv >> 2, tb = qt * 4 + qb;
  if (wv >= 8) {
    const char* kv = ws + WS_KV + head * (64 * 4096);
    const int t = qb * 64 + lane, l16 = lane * 16;
    const unsigned junk = lds_base(smem) + 4 * 32768 + qb * 256;
    const int t2 = t < 196 ? t : 195;
    const int go = (idx * 14 + t2 / 14) * (F_ * 2) + (xcd & 1) * 1792 + (t2 % 14) * 128;
    u32x4 cw[4];
    ring_loader<8, 4, 32768, 8, 0, 2>([&](int s, int so) {
      const unsigned dst = lds_base(smem) + so + qb * 8192;
      const char* src = kv + ((qb >> 1) * 32 + s * 4) * 4096 + (qb & 1) * 8192;
      dma4(src, dst, l16, l16, l16, l16);
      dma4(src + 4096, dst + 4096, l16, l16, l16, l16);
      if (s == 7) { touch(wg, go, junk); touch(wu, go, junk); }
    }, [&](int s) {
      if (bid >= 196) return;
      const int rc = (bid / 28) * 4 + qb, cc = bid % 28;
      if (s == 4) {
        const u16* p = (cc < 14 ? wg : wu) + (rc * 16 + (lane >> 4)) * F_ + (cc < 14 ? cc : cc - 14) * 128 + (lane & 15) * 8;
#pragma unroll
        for (int i = 0; i < 4; ++i) cw[i] = *(const u32x4*)(p + 4 * i * F_);
      }
      if (s == 5) {
        float c[8];
#pragma unroll
        for (int i = 0; i < 4; ++i) {
          c[2 * i] = (bf_lo(cw[0][i]) + bf_lo(cw[1][i])) + (bf_lo(cw[2][i]) + bf_lo(cw[3][i]));
          c[2 * i + 1] = (bf_hi(cw[0][i]) + bf_hi(cw[1][i])) + (bf_hi(cw[2][i]) + bf_hi(cw[3][i]));
        }
#pragma unroll
        for (int i = 0; i < 8; ++i) { c[i] = sum32(c[i]); c[i] += __shfl_xor(c[i], 16); }
        f32x4* d = (f32x4*)(ws + WS_CS) + (rc * 28 + cc) * 32 + (lane & 15) * 2;
        if (lane < 16) {
          st_wt(d, __builtin_bit_cast(u32x4, (f32x4){c[0], c[1], c[2], c[3]}));
          st_wt(d + 1, __builtin_bit_cast(u32x4, (f32x4){c[4], c[5], c[6], c[7]}));
        }
      }
    });
    if (idx < 28) {
      int bs[4];
      gemm_bsrc<0>(bs, qb, lane, nt);
#pragma unroll
      for (int s = 0; s < 4; ++s)
        dma4(wo + s * 112 * D_, lds_base(smem) + (s >> 1) * 65536 + (s & 1) * 16384 + qb * 4096, bs[0], bs[1], bs[2], bs[3]);
    }
    BAR();
    if (idx < 28) {
      BAR();
      spin_until(cnt + mt * 16, 32u);
      const char* A = ws + WS_O + (mt * 2 + (qb >> 1)) * 32768 + (qb & 1) * 4096;
#pragma unroll
      for (int s = 0; s < 4; ++s)
        dma4c(A + s * 8192, lds_base(smem) + 32768 + (s >> 1) * 65536 + (s & 1) * 16384 + qb * 4096, l16);
      WAITV(12); BAR();
      WAITV(8); BAR();
      WAITV(4); BAR();
      WAITV(0); BAR();
    }
    WAITV(0);
    return;
  }
  const char* qp = ws + WS_Q + (head * 64 + tb) * 2048 + lane * 16;
  const u32x4 qf[2] = {*(const u32x4*)qp, *(const u32x4*)(qp + 1024)};
  const unsigned* nn = (const unsigned*)(ws + WS_N) + head * 64 + lane;
  const unsigned nq = nn[0], k2 = wave_max_u(nn[1024]), q2 = wave_max_u((lane >> 2) == qt ? nq : 0u);
  const bool safe = __uint_as_float(k2) * __uint_as_float(q2) <= SAFE_T2;
  f32x16 o = {};
  float m = -3.0e38f;
  TS(2)
  if (safe) attn_loop<true>(smem, qf, o, m, kh);
  else attn_loop<false>(smem, qf, o, m, kh);
  TS(3)
  f32x4* red = (f32x4*)(smem + 32768) + qb * 320 + lane;
  if (kh == 1) {
#pragma unroll
    for (int g = 0; g < 4; ++g) red[g * 64] = (f32x4){o[4 * g], o[4 * g + 1], o[4 * g + 2], o[4 * g + 3]};
    ((float*)(red - lane + 256))[lane] = m;
  }
  WAITL();
  BAR();
  if (kh == 0) {
    float a0 = 1.f, a1 = 1.f;
    if (!safe) {
      const float m1 = ((const float*)(red - lane + 256))[lane], mm = fmaxf(m, m1);
      a0 = __builtin_amdgcn_exp2f(m - mm); a1 = __builtin_amdgcn_exp2f(m1 - mm);
    }
#pragma unroll
    for (int g = 0; g < 4; ++g) {
      const f32x4 t = red[g * 64];
#pragma unroll
      for (int i = 0; i < 4; ++i) o[4 * g + i] = o[4 * g + i] * a0 + t[i] * a1;
    }
    const float inv = 1.0f / halves(o[12])[1];
    unsigned u[8];
#pragma unroll
    for (int i = 0; i < 8; ++i) u[i] = pk2(o[2 * i] * inv, o[2 * i + 1] * inv);
    if (lane >= 32) { u[6] = 0u; u[7] = 0u; }
    char* dst = ws + WS_O + (tb * 16 + head) * 2048 + lane * 16;
    const u32x4 o0 = {u[0], u[1], u[2], u[3]}, o1 = {u[4], u[5], u[6], u[7]};
    asm volatile("global_store_dwordx4 %0, %1, off sc1\n\tglobal_store_dwordx4 %0, %2, off offset:1024 sc1\n\ts_waitcnt vmcnt(0)"
                 ::"v"(dst), "v"(o0), "v"(o1) : "memory");
    if (lane == 0) __hip_atomic_fetch_add(cnt + (tb >> 1) * 16, 1u, __ATOMIC_RELAXED, __HIP_MEMORY_SCOPE_AGENT);
  }
  TS(4)
  if (idx < 28) gemm_body<0>(ws, smem, x, nullptr, mt, nt, wv);
  TSEND(1)
}

constexpr int D_U = 16384 + 32, D_STG = 16384 + 2 * D_U, D_NBUF = 3, D_LDS = D_STG * D_NBUF + 1024;

__global__ __launch_bounds__(512) void k_ffn1(char* __restrict__ ws, const u16* __restrict__ wg, const u16* __restrict__ wu,
                                              const u16* __restrict__ wd) {
  extern __shared__ __attribute__((aligned(16))) char smem[];
  TSDECL
  const int wv = __builtin_amdgcn_readfirstlane(threadIdx.x >> 6), wq = wv & 3;
  const int bid = blockIdx.x, xcd = bid & 7, idx = bid >> 3;
  const int mt = (xcd >> 1) * 4 + idx / 7, nt = (xcd & 1) * 7 + idx % 7;
  const int lane = threadIdx.x & 63, r = lane & 31, h = lane >> 5;
  const int tb = mt * 4 + wq;
  float* cs = (float*)(smem + D_STG * D_NBUF);
  if (wv >= 4) {
    int src[4];
#pragma unroll
    for (int i = 0; i < 4; ++i)
      src[i] = ((wq & 1) * 32 + 4 * i + (lane >> 4)) * (F_ * 2) + nt * 256 + (((lane & 15) ^ ((lane >> 4) << 2)) << 4) - 1024 * i;
    const char* wsrc = (const char*)(wq < 2 ? wg : wu);
    const char* hsrc = ws + WS_H + tb * (28 * 1024);
    const int l16 = lane * 16, t = wq * 64 + lane;
    float cp[28];
    ring_loader<7, D_NBUF, D_STG, 12>([&](int s, int so) {
      const unsigned buf = lds_base(smem) + so + 16384 + (wq >> 1) * D_U + (wq & 1) * 8192;
      dma4(hsrc + s * 4096, lds_base(smem) + so + wq * 4096, l16, l16, l16, l16);
      dma4(wsrc + s * (64 * F_ * 2), buf, src[0], src[1], src[2], src[3]);
      dma4(wsrc + (s * 64 + 16) * (F_ * 2), buf + 4096, src[0], src[1], src[2], src[3]);
    }, [&](int s) {
      if (s == 3) {
        const float* p = (const float*)(ws + WS_CS) + ((t >> 4) & 1) * F_ + nt * 128 + (t >> 5) * 16 + (t & 15);
#pragma unroll
        for (int i = 0; i < 28; ++i) cp[i] = p[i * 2 * F_];
      }
      if (s == 5) {
        const unsigned junk = lds_base(smem) + (5 % D_NBUF) * D_STG + wq * 256;
        touch(wd, (idx * 448 + t) * 128, junk);
        touch(wd, (idx * 448 + 192 + t) * 128, junk);
        WAITV(0);
      }
      if (s == 4) {
        float c = 0.f;
#pragma unroll
        for (int i = 0; i < 28; ++i) c = fma1(cp[i], 1.0f, c);
        cs[t] = c;
      }
    });
    return;
  }
  const f32x2* sp = (const f32x2*)(ws + WS_ST) + (tb * 14 + 7 * h) * 32 + r;
  f32x2 ps[7];
#pragma unroll
  for (int i = 0; i < 7; ++i) ps[i] = sp[i * 32];
  const int q4 = (lane & 15) >> 2, p4 = lane & 3, blk = (lane >> 4) & 1;
  int bo[4];
#pragma unroll
  for (int i = 0; i < 4; ++i) bo[i] = 16384 + blk * D_U + (4 * h + q4) * 256 + ((i ^ q4) << 6) + 8 * p4;
  const char* ap = smem + wq * 4096 + lane * 16;
  f32x16 acc[8];
#pragma unroll
  for (int b = 0; b < 8; ++b)
#pragma unroll
    for (int i = 0; i < 16; ++i) acc[b][i] = 0.f;
  const f32x2 tot = ((ps[0] + ps[1]) + (ps[2] + ps[3])) + ((ps[4] + ps[5]) + ps[6]);
  f32x2 st = ln_scalars(sum32(tot[0]), sum32(tot[1]));
  PIN1(st);
  u32x4 w[2][8], a[2];
  SB();
  BAR();
  TS(2)
  a[0] = lds_r128(ap);
#pragma unroll
  for (int b = 0; b < 8; ++b) w[0][b] = lds_tr2(smem + bo[b >> 1] + (b & 1) * 32, smem + bo[b >> 1] + (b & 1) * 32 + 2048);
  SB();
  int so = 0;
#pragma unroll 1
  for (int s = 0; s < 7; ++s) {
#pragma unroll
    for (int j = 0; j < 4; ++j) {
      const int c = j & 1, n = c ^ 1, jn = (j + 1) & 3;
      __builtin_amdgcn_s_waitcnt(0xc07f);
      if (j == 3) {
        acc[0] = mfma_u(w[c][0], a[c], acc[0]);
        SB();
        if (s < 6) BAR();
        so = so == (D_NBUF - 1) * D_STG ? 0 : so + D_STG;
        SB();
      }
      const char* p = smem + so + jn * 4096;
      a[n] = lds_r128(ap + so + jn * 1024);
#pragma unroll
      for (int b = 0; b < 8; ++b) w[n][b] = lds_tr2(p + bo[b >> 1] + (b & 1) * 32, p + bo[b >> 1] + (b & 1) * 32 + 2048);
#pragma unroll
      for (int b = j == 3 ? 1 : 0; b < 8; ++b) acc[b] = mfma_u(w[c][b], a[c], acc[b]);
      if (j == 3) SG(0x100, 4);
#pragma unroll
      for (int b = 0; b < 5; ++b) { SG(0x008, 1); SG(0x100, 4); }
      SG(0x008, 3);
      SB();
    }
  }
  TS(9)
  const float k1 = st[1] * (-2.0f * 0.7978845608028654f * 1.4426950408889634f);
  const float k3 = k1 * st[1] * st[1] * 0.044715f, i2 = 1.0f / (st[1] * st[1]);
  const f32x2 c1 = {k1, k1}, c3 = {k3, k3}, ir2 = {i2, i2}, nm = {-st[0], -st[0]};
  char* dst = ws + WS_ACT + (size_t)(tb * 112 + nt * 8) * 1024 + lane * 16;
#pragma unroll
  for (int b = 0; b < 8; ++b) {
    u32x4 o;
    f32x4 c4[4];
#pragma unroll
    for (int g = 0; g < 4; ++g) c4[g] = *(const f32x4*)(cs + b * 32 + 8 * g + 4 * h);
#pragma unroll
    for (int i = 0; i < 4; ++i) {
      const int q = i >> 1, e = (i & 1) * 2;
      const f32x2 g = __builtin_elementwise_fma(nm, (f32x2){c4[q][e], c4[q][e + 1]}, (f32x2){acc[b][2 * i], acc[b][2 * i + 1]});
      const f32x2 u = __builtin_elementwise_fma(nm, (f32x2){c4[2 + q][e], c4[2 + q][e + 1]}, (f32x2){acc[b][8 + 2 * i], acc[b][9 + 2 * i]});
      const f32x2 z = __builtin_elementwise_fma(g * g, c3, c1) * g;
      const f32x2 d = __builtin_elementwise_fma((f32x2){__builtin_amdgcn_exp2f(z[0]), __builtin_amdgcn_exp2f(z[1])}, ir2, ir2);
      const f32x2 v = (g * u) * (f32x2){__builtin_amdgcn_rcpf(d[0]), __builtin_amdgcn_rcpf(d[1])};
      o[i] = pk2(v[0], v[1]);
    }
    st_wt(dst + b * 1024, o);
  }
  TSEND(3)
}

constexpr int C_LDS = 4 * 32768;
__global__ __launch_bounds__(768) void k_down(char* __restrict__ ws, const u16* __restrict__ B, u16* __restrict__ out) {
  extern __shared__ __attribute__((aligned(16))) char smem[];
  TSDECL
  const int lane = threadIdx.x & 63, wv = __builtin_amdgcn_readfirstlane(threadIdx.x >> 6);
  const int bid = blockIdx.x, xcd = bid & 7, idx = bid >> 3;
  const int mt = xcd * 4 + idx / 7, nt = idx % 7;
  if (wv >= 8) {
    const int lw = wv - 8, l16 = lane * 16;
    const char* A = ws + WS_ACT + (size_t)(mt * 2 + (lw >> 1)) * 112 * 1024 + (lw & 1) * 4096;
    int bs[4];
    gemm_bsrc<1>(bs, lw, lane, nt);
    ring_loader<14, 4, 32768, 8>([&](int s, int so) {
      const unsigned buf = lds_base(smem) + so + lw * 4096;
      dma4(B + s * 128 * D_, buf + 16384, bs[0], bs[1], bs[2], bs[3]);
      dma4(A + s * 8192, buf, l16, l16, l16, l16);
    });
    return;
  }
  gemm_body<1>(ws, smem, nullptr, out, mt, nt, wv);
  TSEND(4)
}

__global__ __launch_bounds__(768) void k_qkv(const u16* __restrict__ x, const u16* __restrict__ w, char* __restrict__ ws) {
  extern __shared__ __attribute__((aligned(16))) char smem[];
  const int bid = blockIdx.x, xcd = bid & 7, idx = bid >> 3;
  const int mt = (xcd >> 1) * 4 + (idx >> 3), nt = (xcd & 1) * 8 + (idx & 7);
  const int wv = __builtin_amdgcn_readfirstlane(threadIdx.x >> 6), wq = wv & 3;
  if (wv >= 8) {
    const int lane = threadIdx.x & 63, t = wq * 64 + lane;
    if (bid == 0 && t < 32) __hip_atomic_store((unsigned*)(ws + WS_CNT) + t * 16, 0u, __ATOMIC_RELAXED, __HIP_MEMORY_SCOPE_AGENT);
    const int ca = (84 * nt) & ~7;
    int sw[3], sx[4];
#pragma unroll
    for (int i = 0; i < 3; ++i) {
      int ci = 64 * (3 * wq + i) + lane, row = ci / 12, c = ci - row * 12;
      int col = ca + 8 * c;
      col = col > (N3_ - 8) ? (N3_ - 8) : col;
      sw[i] = (row * N3_ + col) * 2 - 1024 * i;
    }
#pragma unroll
    for (int i = 0; i < 4; ++i) {
      int c = (lane & 7) ^ (((i & 1) * 4 + (lane >> 4)) & 7);
      sx[i] = ((mt * 128 + 32 * wq + 8 * i + (lane >> 3)) * D_ + 8 * c) * 2 - 1024 * i;
    }
    ring_loader<7, A_NBUF, A_STG, 7>([&](int s, int so) {
      dma3(w + 64 * s * N3_, lds_base(smem) + so + A_XB + 3 * wq * 1024, sw[0], sw[1], sw[2]);
      dma4(x + 64 * s, lds_base(smem) + so + wq * 4096, sx[0], sx[1], sx[2], sx[3]);
    });
    BAR();
    float* csp = (float*)(smem + A_MISC);
    f32x2* tst = (f32x2*)(smem + A_MISC + 5 * 96 * 4);
    if (t < 96) csp[384 + t] = csp[t] + csp[96 + t] + csp[192 + t] + csp[288 + t];
    if (t >= 128) {
      const f32x2 p = tst[t] + tst[t + 128];
      tst[t - 128] = ln_scalars(p[0], p[1]);
    }
    WAITL();
    BAR();
    return;
  }
  if (wv < 5) {
    __builtin_amdgcn_s_sleep(TSLEEP);
    const int t = threadIdx.x;
    const unsigned junk = lds_base(smem) + A_LDS - 256;
    const int t1 = t < 112 ? t : 111, t2 = t - 128 < 154 ? t - 128 : 153;
    if (wv < 2) touch(x, ((xcd >> 1) * 512 + idx * 16) * (D_ * 2) + t1 * 128, junk);
    else touch(w, (idx * 14 + t2 / 11) * (N3_ * 2) + ((xcd & 1) * 10 + t2 % 11) * 128, junk);
  }
  if (wv == 5) {
    const char *c0 = (const char*)&mark_lo, *c1 = (const char*)&mark_hi;
    const char* lo = c0 < c1 ? c0 : c1;
    const int len = (int)(c0 < c1 ? c1 - c0 : c0 - c1);
    int off = (idx * 32 + (threadIdx.x & 31)) * 128;
    off = off + 4 <= len ? off : 0;
    touch(lo, off, lds_base(smem) + A_LDS - 256);
  }
  if (wv < 4) {
    if (nt >= 11) qkv_body<0, true>(ws, smem, mt, nt, wq);
    else qkv_body<0, false>(ws, smem, mt, nt, wq);
  } else {
    if (nt >= 10) qkv_body<1, true>(ws, smem, mt, nt, wq);
    else qkv_body<1, false>(ws, smem, mt, nt, wq);
  }
}

__device__ __noinline__ void mark_hi() { asm volatile("s_nop 1"); }

extern "C" void kernel_launch(void* const* d_in, const int* in_sizes, int n_in, void* d_out, int out_size, void* d_ws,
                              size_t ws_size, hipStream_t stream) {
  const u16* x = (const u16*)d_in[0];
  const u16* wqkv = (const u16*)d_in[1];
  const u16* wo = (const u16*)d_in[2];
  const u16* wg = (const u16*)d_in[4];
  const u16* wu = (const u16*)d_in[5];
  const u16* wd = (const u16*)d_in[6];
  u16* out = (u16*)d_out;
  char* ws = (char*)d_ws;
  (void)hipFuncSetAttribute((const void*)k_qkv, hipFuncAttributeMaxDynamicSharedMemorySize, A_LDS);
  (void)hipFuncSetAttribute((const void*)k_attn, hipFuncAttributeMaxDynamicSharedMemorySize, B_LDS);
  (void)hipFuncSetAttribute((const void*)k_down, hipFuncAttributeMaxDynamicSharedMemorySize, C_LDS);
  (void)hipFuncSetAttribute((const void*)k_ffn1, hipFuncAttributeMaxDynamicSharedMemorySize, D_LDS);
  k_qkv<<<256, 768, A_LDS, stream>>>(x, wqkv, ws);
  k_attn<<<256, 768, B_LDS, stream>>>(ws, wo, wg, wu, x);
  k_ffn1<<<224, 512, D_LDS, stream>>>(ws, wg, wu, wd);
  k_down<<<224, 768, C_LDS, stream>>>(ws, wd, out);
}
```
